```python
import torch

def _softmax(x, axis=-1):
    return torch.softmax(x, dim=axis)
def _gelu(x):
    return torch.nn.functional.gelu(x, approximate="tanh")
def _silu(x):
    return torch.nn.functional.silu(x)
def _sigmoid(x):
    return torch.sigmoid(x)
def _layernorm(x, axis=-1, eps=1e-5):
    m = x.mean(dim=axis, keepdim=True)
    v = x.var(dim=axis, keepdim=True, unbiased=False)
    return (x - m) / torch.sqrt(v + eps)
def _scatter_add(tgt, idx, src, axis=0):
    return tgt.clone().index_add_(axis, idx, src)
def _segment_sum(src, seg, n):
    out = torch.zeros((n,) + src.shape[1:], dtype=src.dtype)
    return out.index_add_(0, seg, src)
def _pad(x, pw):
    flat = tuple(v for pair in reversed(pw) for v in pair)
    return torch.nn.functional.pad(x, flat)

kernel_name = 'standalone__ssm_conv_s4d__prompt'

def setup_inputs():

    _w = (lambda a: (lambda t: t.to(torch.bfloat16) if t.is_floating_point() else t)(torch.as_tensor(a)))
    Bsz, L, D, N = 1, 2048, 448, 256
    return {
        "u": _w((lambda *s: torch.randn(*s, dtype=torch.bfloat16) * 0.05)(Bsz, L, D)),
        "log_A_real": _w((lambda *s: torch.randn(*s, dtype=torch.bfloat16) * 6.4)(D, N) * 0.5),
        "A_imag": _w((lambda *s: torch.randn(*s, dtype=torch.bfloat16) * 0.05)(D, N) * 3.14),
        "C_real": _w((lambda *s: torch.randn(*s, dtype=torch.bfloat16) * 0.05)(D, N) / N ** 0.5),
        "C_imag": _w((lambda *s: torch.randn(*s, dtype=torch.bfloat16) * 0.05)(D, N) / N ** 0.5),
        "log_dt": _w((lambda *s: torch.randn(*s, dtype=torch.bfloat16) * 0.05)(D) * 0.5 - 4.0),
        "D_skip": _w((lambda *s: torch.randn(*s, dtype=torch.bfloat16) * 0.05)(D)),
    }

def reference(u, log_A_real, A_imag, C_real, C_imag, log_dt, D_skip):
    L = u.shape[1]
    dt = torch.exp(log_dt)
    A = -torch.exp(log_A_real) + 1j * A_imag
    C = (C_real + 1j * C_imag) * dt[:, None]
    pos = (lambda x, dt: x.to(dt))(torch.arange(L), log_dt.dtype)
    P = torch.exp((A * dt[:, None])[:, :, None] * pos[None, None, :])
    K = 2.0 * torch.einsum("dn,dnl->dl", C, P).real
    L2 = 2 * L
    u_t = (lambda x, *d: x.permute(*d))(u, 0, 2, 1)
    Kf = (lambda x, *a, **k: torch.fft.rfft(x.float(), *a, **k))(K, L2)
    Uf = (lambda x, *a, **k: torch.fft.rfft(x.float(), *a, **k))(u_t, L2)
    y = (lambda x, *a, **k: torch.fft.irfft(x, *a, **k).to(torch.bfloat16))(Uf * Kf[None, :, :], L2)[:, :, :L]
    y = (lambda x, *d: x.permute(*d))(y, 0, 2, 1)
    return y + u * D_skip[None, None, :]
```

```cpp
#include <hip/hip_runtime.h>
#include <hip/hip_bf16.h>

#define L_ 2048
#define D_ 448
#define N_ 256

__device__ __forceinline__ float bf2f(unsigned short x){ return __uint_as_float(((unsigned)x)<<16); }
__device__ __forceinline__ float rbf(float x){ return __bfloat162float(__float2bfloat16(x)); }

__global__ void kgen(const unsigned short* logA, const unsigned short* Aim, const unsigned short* Cr, const unsigned short* Ci,
                     const unsigned short* logdt, float* K){
  int d = blockIdx.x;
  __shared__ float sa[N_], sb[N_], scr[N_], sci[N_];
  float dt = rbf(expf(bf2f(logdt[d])));
  for (int n = threadIdx.x; n < N_; n += blockDim.x){
    float e = rbf(expf(bf2f(logA[d*N_+n])));
    sa[n] = -e*dt; sb[n] = bf2f(Aim[d*N_+n])*dt;
    scr[n] = bf2f(Cr[d*N_+n])*dt; sci[n] = bf2f(Ci[d*N_+n])*dt;
  }
  __syncthreads();
  for (int l = threadIdx.x; l < L_; l += blockDim.x){
    float p = rbf((float)l);
    float acc = 0.f;
    for (int n = 0; n < N_; n++){
      float m = expf(sa[n]*p); float s,c; sincosf(sb[n]*p,&s,&c);
      acc += m*(scr[n]*c - sci[n]*s);
    }
    K[d*L_+l] = 2.f*acc;
  }
}

__global__ void conv(const unsigned short* u, const float* K, const unsigned short* Dsk, __hip_bfloat16* out){
  int d = blockIdx.x;
  __shared__ float su[L_], sk[L_];
  for (int l = threadIdx.x; l < L_; l += blockDim.x){ su[l] = bf2f(u[l*D_+d]); sk[l] = K[d*L_+l]; }
  __syncthreads();
  float Dv = bf2f(Dsk[d]);
  for (int l = threadIdx.x; l < L_; l += blockDim.x){
    float acc = 0.f;
    for (int j = 0; j <= l; j++) acc += sk[j]*su[l-j];
    float y = rbf(acc);
    float s = rbf(su[l]*Dv);
    out[l*D_+d] = __float2bfloat16(y+s);
  }
}

extern "C" void kernel_launch(void* const* d_in, const int* in_sizes, int n_in,
                              void* d_out, int out_size, void* d_ws, size_t ws_size,
                              hipStream_t stream) {
  const unsigned short* u = (const unsigned short*)d_in[0];
  float* K = (float*)d_ws;
  kgen<<<D_, 256, 0, stream>>>((const unsigned short*)d_in[1], (const unsigned short*)d_in[2], (const unsigned short*)d_in[3], (const unsigned short*)d_in[4], (const unsigned short*)d_in[5], K);
  conv<<<D_, 256, 0, stream>>>(u, K, (const unsigned short*)d_in[6], (__hip_bfloat16*)d_out);
}
```

```cpp
#include <hip/hip_runtime.h>
#include <stdint.h>

#define L_ 2048
#define D_ 448
#define N_ 256

typedef _Float16 h8 __attribute__((ext_vector_type(8)));
typedef _Float16 h2 __attribute__((ext_vector_type(2)));
typedef __bf16 b8 __attribute__((ext_vector_type(8)));
typedef __bf16 b2 __attribute__((ext_vector_type(2)));
typedef float f2 __attribute__((ext_vector_type(2)));
typedef float f4 __attribute__((ext_vector_type(4)));
typedef float f16v __attribute__((ext_vector_type(16)));
typedef unsigned u2 __attribute__((ext_vector_type(2)));
typedef unsigned u4 __attribute__((ext_vector_type(4)));
#define LAS __attribute__((address_space(3)))

constexpr int TAB_STRIDE = 272;
constexpr int TAB_REGION = 32 * TAB_STRIDE;
constexpr int U_OFF   = 8 * TAB_REGION;
constexpr int U_COPY  = 4352;
constexpr int U_CH    = 8 * U_COPY;
constexpr int RAW_OFF = U_OFF + 2 * U_CH;
constexpr int FLAG_OFF = RAW_OFF + 8192;
constexpr int LDS_BYTES = FLAG_OFF + 64;
constexpr int KC_OFF = 4096;
constexpr int R_OFF  = 4096;

#define STAMP(i) do {} while (0)

__device__ __forceinline__ float bf2f(unsigned x) { return __uint_as_float(x << 16); }
__device__ __forceinline__ unsigned pk_bf16(float a, float b) { f2 v = {a, b}; b2 r = __builtin_convertvector(v, b2); return __builtin_bit_cast(unsigned, r); }
__device__ __forceinline__ unsigned pk_f16(f2 v) { h2 r = __builtin_convertvector(v, h2); return __builtin_bit_cast(unsigned, r); }
__device__ __forceinline__ float rbf(float x) { return __uint_as_float(pk_bf16(x, x) << 16); }
__device__ __forceinline__ float exp_acc(float x) {
  const float L2E = 1.44269502162933349609375f, L2E_LO = 1.925963033500011e-8f;
  float t = x * L2E;
  float r = __builtin_fmaf(x, L2E, -t);
  r = __builtin_fmaf(x, L2E_LO, r);
  float e = __builtin_amdgcn_exp2f(t);
  return __builtin_fmaf(e, r * 0.693147180559945f, e);
}
__device__ __forceinline__ f2 cmul(f2 x, f2 c) {
  f2 t, r;
  asm("v_pk_mul_f32 %0, %1, %2 op_sel_hi:[1,0]" : "=v"(t) : "v"(x), "v"(c));
  asm("v_pk_fma_f32 %0, %1, %2, %3 op_sel:[1,1,0] op_sel_hi:[0,1,1] neg_lo:[1,0,0]" : "=v"(r) : "v"(x), "v"(c), "v"(t));
  return r;
}
__device__ __forceinline__ f2 cexp_p(float a2, float th, float p) {
  float m = __builtin_amdgcn_exp2f(a2 * p);
  float f = __builtin_amdgcn_fractf(th * p);
  f2 r = {m * __builtin_amdgcn_cosf(f), m * __builtin_amdgcn_sinf(f)};
  return r;
}
__device__ __forceinline__ int copy_base(int sg) {
  const int tau = (sg + 7) & 7;
  const int o = min(4 * (tau >> 1) + (tau & 1), 12);
  return sg * U_COPY + 16 * o;
}
__device__ __forceinline__ void lds_signal(LAS unsigned* p) {
  asm volatile("s_waitcnt lgkmcnt(0)" ::: "memory");
  if ((threadIdx.x & 63) == 0) __hip_atomic_fetch_add(p, 1u, __ATOMIC_RELAXED, __HIP_MEMORY_SCOPE_WORKGROUP);
  asm volatile("" ::: "memory");
}
__device__ __forceinline__ void lds_wait(LAS unsigned* p, unsigned n) {
  asm volatile("" ::: "memory");
  while ((unsigned)__builtin_amdgcn_readfirstlane(__hip_atomic_load(p, __ATOMIC_RELAXED, __HIP_MEMORY_SCOPE_WORKGROUP)) < n)
    __builtin_amdgcn_s_sleep(1);
  asm volatile("" ::: "memory");
}

__global__ __launch_bounds__(768, 3) void s4d_kernel(const unsigned* __restrict__ u32p, const unsigned short* __restrict__ logA,
                                                     const unsigned short* __restrict__ Aim, const unsigned short* __restrict__ Cr,
                                                     const unsigned short* __restrict__ Ci, const unsigned short* __restrict__ logdt,
                                                     const unsigned short* __restrict__ Dsk, unsigned* __restrict__ out
) {
  __shared__ __attribute__((aligned(16))) char smem[LDS_BYTES];
  LAS char* lds = (LAS char*)smem;
  LAS unsigned* flags = (LAS unsigned*)(lds + FLAG_OFF);
  const int b = blockIdx.x;
  const int cp = (b & 7) * 28 + (b >> 3);
  const int lane = threadIdx.x & 63;
  const int w = __builtin_amdgcn_readfirstlane(threadIdx.x >> 6);
  STAMP(0);

  if (w >= 8) {
    const int j = w - 8;
    unsigned dv[2][4];
    unsigned pe[2][4];
    if (j == 0) {
      if (lane < 16) flags[lane] = 0;
      *(LAS u2*)(lds + U_OFF + (lane >> 5) * U_CH + copy_base((lane >> 2) & 7) + (lane & 3) * 8) = u2{0u, 0u};
    }
    asm volatile("s_waitcnt lgkmcnt(0)" ::: "memory");
    __builtin_amdgcn_s_barrier();
    asm volatile("" ::: "memory");
#pragma unroll
    for (int r = 0; r < 2; r++) {
      const int c = j + 4 * r;
      const unsigned* p = u32p + (size_t)(256 * c + 4 * lane) * (D_ / 2) + cp;
#pragma unroll
      for (int i = 0; i < 4; i++) dv[r][i] = p[i * (D_ / 2)];
    }
#pragma unroll
    for (int r = 0; r < 2; r++) {
      const int c = j + 4 * r;
#pragma unroll
      for (int i = 0; i < 4; i++) pe[r][i] = (c > 0) ? u32p[(size_t)(256 * c - 4 + i) * (D_ / 2) + cp] : 0u;
    }
#pragma unroll
    for (int r = 0; r < 2; r++) {
      const int c = j + 4 * r;
      const unsigned d0 = dv[r][0], d1 = dv[r][1], d2 = dv[r][2], d3 = dv[r][3];
      *(LAS u4*)(lds + RAW_OFF + (256 * c + 4 * lane) * 4) = u4{d0, d1, d2, d3};
      const int m0 = 256 * c + 4 * lane + 16;
#pragma unroll
      for (int ch = 0; ch < 2; ch++) {
        const unsigned sel = ch ? 0x07060302u : 0x05040100u;
        const unsigned p0 = __builtin_amdgcn_perm(d1, d0, sel), p1 = __builtin_amdgcn_perm(d3, d2, sel);
        const unsigned q0 = __builtin_amdgcn_perm(pe[r][1], pe[r][0], sel), q1 = __builtin_amdgcn_perm(pe[r][3], pe[r][2], sel);
        const unsigned P0 = __builtin_amdgcn_update_dpp(q0, p0, 0x138, 0xf, 0xf, false);
        const unsigned P1 = __builtin_amdgcn_update_dpp(q1, p1, 0x138, 0xf, 0xf, false);
        const unsigned a0 = __builtin_amdgcn_alignbit(P1, P0, 16), a1 = __builtin_amdgcn_alignbit(p0, P1, 16), a2 = __builtin_amdgcn_alignbit(p1, p0, 16);
        LAS char* base = lds + U_OFF + ch * U_CH;
        base += 2 * m0;
        *(LAS u2*)(base + copy_base(0)) = u2{p0, p1};
        *(LAS u2*)(base + copy_base(1) - 8) = u2{a0, a1};
        *(LAS u2*)(base + copy_base(2) - 8) = u2{P1, p0};
        *(LAS u2*)(base + copy_base(3) - 8) = u2{a1, a2};
        *(LAS u2*)(base + copy_base(4) - 8) = u2{p0, p1};
        *(LAS u2*)(base + copy_base(5) - 16) = u2{a0, a1};
        *(LAS u2*)(base + copy_base(6) - 16) = u2{P1, p0};
        *(LAS u2*)(base + copy_base(7) - 16) = u2{a1, a2};
      }
      lds_signal(flags + 8 + c);
      STAMP(1 + r);
    }
    return;
  }

  const int ch = w >> 2, mg = w & 3;
  const int d = 2 * cp + ch;
  const int pidx = d * N_ + 64 * mg + lane;
  const unsigned la = logA[pidx], ai = Aim[pidx], cr = Cr[pidx], ci = Ci[pidx];
  const unsigned ldt = logdt[d];
  const unsigned ds0 = Dsk[2 * cp], ds1 = Dsk[2 * cp + 1];
  __builtin_amdgcn_s_barrier();
  asm volatile("" ::: "memory");

  LAS char* tab = lds + w * TAB_REGION;
  float unscale;
  f16v acc;
  {
    const float dt = rbf(exp_acc(bf2f(ldt)));
    const float e = rbf(exp_acc(bf2f(la)));
    const float a2 = -(e * dt) * 1.44269504088896f;
    const float th = (bf2f(ai) * dt) * 0.159154943091895f;
    f2 C = {bf2f(cr) * dt, bf2f(ci) * dt};
    unsigned mx = max(__float_as_uint(C[0]) & 0x7fffffffu, __float_as_uint(C[1]) & 0x7fffffffu);
    mx = max(mx, (unsigned)__builtin_amdgcn_update_dpp(0, (int)mx, 0x111, 0xf, 0xf, true));
    mx = max(mx, (unsigned)__builtin_amdgcn_update_dpp(0, (int)mx, 0x112, 0xf, 0xf, true));
    mx = max(mx, (unsigned)__builtin_amdgcn_update_dpp(0, (int)mx, 0x114, 0xf, 0xf, true));
    mx = max(mx, (unsigned)__builtin_amdgcn_update_dpp(0, (int)mx, 0x118, 0xf, 0xf, true));
    mx = max(mx, (unsigned)__builtin_amdgcn_update_dpp(0, (int)mx, 0x142, 0xa, 0xf, true));
    mx = max(mx, (unsigned)__builtin_amdgcn_update_dpp(0, (int)mx, 0x143, 0xc, 0xf, true));
    unsigned E = (unsigned)__builtin_amdgcn_readlane((int)mx, 63) >> 23;
    E = min(max(E, 1u), 253u);
    const float sc = __uint_as_float((254u - E) << 23);
    unscale = __uint_as_float((E + 1u) << 23);
    C[0] *= sc; C[1] *= sc;
    STAMP(1);

    const f2 w1 = cexp_p(a2, th, 1.0f);
    f2 v = cexp_p(a2, th, 8.0f);
    const f2 w256 = cexp_p(a2, th, 256.0f);
    const f2 w2 = cmul(w1, w1), w4 = cmul(w2, w2);
    const f2 w512 = cmul(w256, w256), w1024 = cmul(w512, w512);
    v[1] = -v[1];
    const f2 v2 = cmul(v, v);
    LAS unsigned* tw = (LAS unsigned*)(tab + lane * 4);
    constexpr int RS = TAB_STRIDE / 4;
    const LAS char* ta = tab + (lane & 31) * TAB_STRIDE + (lane >> 5) * 16;
    h8 Bf[8];
    {
      tw[0 * RS] = 0x00003c00u;
      f2 ya = v, yb = v2;
      tw[1 * RS] = pk_f16(ya);
      tw[2 * RS] = pk_f16(yb);
#pragma unroll
      for (int l = 3; l < 32; l += 2) {
        ya = cmul(ya, v2);
        tw[l * RS] = pk_f16(ya);
        if (l + 1 < 32) { yb = cmul(yb, v2); tw[(l + 1) * RS] = pk_f16(yb); }
      }
#pragma unroll
      for (int ks = 0; ks < 8; ks++) Bf[ks] = *(const LAS h8*)(ta + ks * 32);
    }
    {
      f2 x = C;
      tw[0 * RS] = pk_f16(x);
#pragma unroll
      for (int jj = 1; jj < 8; jj++) { x = cmul(x, w1); tw[jj * RS] = pk_f16(x); }
      x = cmul(C, w256);
      tw[8 * RS] = pk_f16(x);
#pragma unroll
      for (int jj = 9; jj < 12; jj++) { x = cmul(x, w2); tw[jj * RS] = pk_f16(x); }
      x = cmul(C, w512);
      tw[12 * RS] = pk_f16(x);
      tw[13 * RS] = pk_f16(cmul(x, w4));
      x = cmul(x, w256);
      tw[14 * RS] = pk_f16(x);
      tw[15 * RS] = pk_f16(cmul(x, w4));
      x = cmul(C, w1024);
      tw[16 * RS] = pk_f16(x);
#pragma unroll
      for (int jj = 17; jj < 21; jj++) { x = cmul(x, w256); tw[jj * RS] = pk_f16(x); }
    }
    STAMP(2);
#pragma unroll
    for (int i = 0; i < 16; i++) acc[i] = 0.f;
#pragma unroll
    for (int ks = 0; ks < 8; ks++) {
      const h8 A = *(const LAS h8*)(ta + ks * 32);
      acc = __builtin_amdgcn_mfma_f32_32x32x16_f16(A, Bf[ks], acc, 0, 0, 0);
    }
  }
  STAMP(3);
  {
    LAS float* pw = (LAS float*)(tab + lane * 4);
#pragma unroll
    for (int r = 0; r < 12; r++) pw[r * 64] = acc[r] * unscale;
    lds_signal(flags + 0 + ch);
    lds_wait(flags + 0 + ch, 4);
    LAS float* kc = (LAS float*)(lds + (4 * ch) * TAB_REGION + KC_OFF);
    const int col = lane & 31, h = lane >> 5;
    const LAS char* pr = lds + (4 * ch) * TAB_REGION + (3 * mg) * 256 + lane * 4;
    float sum[3];
#pragma unroll
    for (int i = 0; i < 3; i++) {
      const float p0 = *(const LAS float*)(pr + i * 256), p1 = *(const LAS float*)(pr + i * 256 + TAB_REGION);
      const float p2 = *(const LAS float*)(pr + i * 256 + 2 * TAB_REGION), p3 = *(const LAS float*)(pr + i * 256 + 3 * TAB_REGION);
      sum[i] = (p0 + p1) + (p2 + p3);
    }
#pragma unroll
    for (int i = 0; i < 3; i++) {
      const int r = 3 * mg + i, rq = r >> 2, rl = r & 3;
      const int b0 = rq == 0 ? rl : (rq == 1 ? 256 + rl : 512 + 32 * rl);
      const int s0 = rq == 0 ? 8 : (rq == 1 ? 4 : 1);
      const int b1 = rq == 0 ? rl + 4 : (rq == 1 ? 384 + 64 * (rl >> 1) + (rl & 1) : (rl == 0 ? 640 : 648));
      const int s1 = rq == 0 ? 8 : (rq == 1 ? 2 : 1);
      const int idx = (h ? b1 : b0) + (h ? s1 : s0) * col;
      kc[idx] = sum[i];
    }
    lds_signal(flags + 2 + ch);
    lds_wait(flags + 2 + ch, 4);
    STAMP(4);
    const int q = 255 - (64 * mg + lane);
    float kv[8];
#pragma unroll
    for (int i = 0; i < 8; i++) {
      const int jpos = 8 * q + i;
      const unsigned bits = pk_bf16((float)jpos, 0.f) & 0xffffu;
      const int idx = (jpos < 128) ? jpos : (int)bits - 17024;
      kv[i] = kc[idx];
    }
    LAS char* R = lds + (4 * ch + 1) * TAB_REGION + R_OFF;
    *(LAS u4*)(R + (64 * mg + lane) * 16) = u4{pk_bf16(kv[7], kv[6]), pk_bf16(kv[5], kv[4]), pk_bf16(kv[3], kv[2]), pk_bf16(kv[1], kv[0])};
    if (mg == 0 && lane < 32) *(LAS u4*)(R + 4096 + lane * 16) = u4{0u, 0u, 0u, 0u};
    lds_signal(flags + 4 + ch);
  }
  lds_wait(flags + 4, 4);
  lds_wait(flags + 5, 4);
  STAMP(5);

  {
    const int t = (w < 4) ? 7 - w : w - 4;
    const int a = lane & 15, g = lane >> 4;
    const int sg = (a + 1) & 7;
    const LAS char* pa = lds + U_OFF + copy_base(sg) + 2 * (8 * g + a + 1 - sg);
    const LAS char* pb0 = lds + 1 * TAB_REGION + R_OFF + 4064 - 512 * t - 32 * a + 16 * g;
    const LAS char* pb1 = lds + 5 * TAB_REGION + R_OFF + 4064 - 512 * t - 32 * a + 16 * g;
    f4 c0 = {0.f, 0.f, 0.f, 0.f}, c1 = {0.f, 0.f, 0.f, 0.f};
    for (int m = 0; m <= t; m++) {
      lds_wait(flags + 8 + m, 1);
#pragma unroll
      for (int s = 0; s < 8; s++) {
        const b8 A0 = *(const LAS b8*)(pa + 64 * s);
        const b8 A1 = *(const LAS b8*)(pa + U_CH + 64 * s);
        const b8 B0 = *(const LAS b8*)(pb0 + 64 * s);
        const b8 B1 = *(const LAS b8*)(pb1 + 64 * s);
        c0 = __builtin_amdgcn_mfma_f32_16x16x32_bf16(A0, B0, c0, 0, 0, 0);
        c1 = __builtin_amdgcn_mfma_f32_16x16x32_bf16(A1, B1, c1, 0, 0, 0);
      }
      pa += 512; pb0 += 512; pb1 += 512;
    }
    STAMP(6);
    const int l0 = 256 * t + 16 * a + 4 * g;
    const u4 uu = *(const LAS u4*)(lds + RAW_OFF + l0 * 4);
    const float D0 = bf2f(ds0), D1 = bf2f(ds1);
    unsigned* op = out + (size_t)l0 * (D_ / 2) + cp;
#pragma unroll
    for (int r = 0; r < 4; r++) {
      const unsigned y = pk_bf16(c0[r], c1[r]);
      const unsigned sk = pk_bf16(__uint_as_float(uu[r] << 16) * D0, __uint_as_float(uu[r] & 0xffff0000u) * D1);
      const float o0 = __uint_as_float(y << 16) + __uint_as_float(sk << 16);
      const float o1 = __uint_as_float(y & 0xffff0000u) + __uint_as_float(sk & 0xffff0000u);
      op[r * (D_ / 2)] = pk_bf16(o0, o1);
    }
    STAMP(7);
  }
}

extern "C" void kernel_launch(void* const* d_in, const int* in_sizes, int n_in, void* d_out, int out_size, void* d_ws,
                              size_t ws_size, hipStream_t stream) {
  s4d_kernel<<<224, 768, 0, stream>>>((const unsigned*)d_in[0], (const unsigned short*)d_in[1], (const unsigned short*)d_in[2],
                                      (const unsigned short*)d_in[3], (const unsigned short*)d_in[4], (const unsigned short*)d_in[5],
                                      (const unsigned short*)d_in[6], (unsigned*)d_out
  );
}
```

```cpp
#include <hip/hip_runtime.h>
#include <stdint.h>

#define L_ 2048
#define D_ 448
#define N_ 256

typedef _Float16 h8 __attribute__((ext_vector_type(8)));
typedef _Float16 h2 __attribute__((ext_vector_type(2)));
typedef __bf16 b8 __attribute__((ext_vector_type(8)));
typedef __bf16 b2 __attribute__((ext_vector_type(2)));
typedef float f2 __attribute__((ext_vector_type(2)));
typedef float f4 __attribute__((ext_vector_type(4)));
typedef float f16v __attribute__((ext_vector_type(16)));
typedef unsigned u2 __attribute__((ext_vector_type(2)));
typedef unsigned u4 __attribute__((ext_vector_type(4)));
#define LAS __attribute__((address_space(3)))

constexpr int TAB_STRIDE = 272;
constexpr int TAB_REGION = 32 * TAB_STRIDE;
constexpr int U_OFF   = 8 * TAB_REGION;
constexpr int U_COPY  = 4352;
constexpr int U_CH    = 8 * U_COPY;
constexpr int RAW_OFF = U_OFF + 2 * U_CH;
constexpr int FLAG_OFF = RAW_OFF + 8192;
constexpr int LDS_BYTES = FLAG_OFF + 64;
constexpr int KC_OFF = 4096;
constexpr int R_OFF  = 4096;

#define STAMP(i) do { if ((i) == 1) tsc = __builtin_amdgcn_s_memrealtime(); } while (0)
#define RSTAMP(i) do {} while (0)

__device__ __forceinline__ float bf2f(unsigned x) { return __uint_as_float(x << 16); }
__device__ __forceinline__ unsigned pk_bf16(float a, float b) { f2 v = {a, b}; b2 r = __builtin_convertvector(v, b2); return __builtin_bit_cast(unsigned, r); }
__device__ __forceinline__ unsigned pk_f16(f2 v) { h2 r = __builtin_convertvector(v, h2); return __builtin_bit_cast(unsigned, r); }
__device__ __forceinline__ float rbf(float x) { return __uint_as_float(pk_bf16(x, x) << 16); }
__device__ __forceinline__ float exp_acc(float x) {
  const float L2E = 1.44269502162933349609375f, L2E_LO = 1.925963033500011e-8f;
  float t = x * L2E;
  float r = __builtin_fmaf(x, L2E, -t);
  r = __builtin_fmaf(x, L2E_LO, r);
  float e = __builtin_amdgcn_exp2f(t);
  return __builtin_fmaf(e, r * 0.693147180559945f, e);
}
__device__ __forceinline__ f2 cmul(f2 x, f2 c) {
  f2 t, r;
  asm("v_pk_mul_f32 %0, %1, %2 op_sel_hi:[1,0]" : "=v"(t) : "v"(x), "v"(c));
  asm("v_pk_fma_f32 %0, %1, %2, %3 op_sel:[1,1,0] op_sel_hi:[0,1,1] neg_lo:[1,0,0]" : "=v"(r) : "v"(x), "v"(c), "v"(t));
  return r;
}
__device__ __forceinline__ f2 cexp_p(float a2, float th, float p) {
  float m = __builtin_amdgcn_exp2f(a2 * p);
  float f = __builtin_amdgcn_fractf(th * p);
  f2 r = {m * __builtin_amdgcn_cosf(f), m * __builtin_amdgcn_sinf(f)};
  return r;
}
__device__ __forceinline__ int copy_base(int sg) {
  const int tau = (sg + 7) & 7;
  const int o = min(4 * (tau >> 1) + (tau & 1), 12);
  return sg * U_COPY + 16 * o;
}
__device__ __forceinline__ void lds_signal(LAS unsigned* p) {
  asm volatile("s_waitcnt lgkmcnt(0)" ::: "memory");
  if ((threadIdx.x & 63) == 0) __hip_atomic_fetch_add(p, 1u, __ATOMIC_RELAXED, __HIP_MEMORY_SCOPE_WORKGROUP);
  asm volatile("" ::: "memory");
}
__device__ __forceinline__ void lds_wait(LAS unsigned* p, unsigned n) {
  asm volatile("" ::: "memory");
  while ((unsigned)__builtin_amdgcn_readfirstlane(__hip_atomic_load(p, __ATOMIC_RELAXED, __HIP_MEMORY_SCOPE_WORKGROUP)) < n)
    __builtin_amdgcn_s_sleep(1);
  asm volatile("" ::: "memory");
}

__global__ __launch_bounds__(768, 3) void s4d_kernel(const unsigned* __restrict__ u32p, const unsigned short* __restrict__ logA,
                                                     const unsigned short* __restrict__ Aim, const unsigned short* __restrict__ Cr,
                                                     const unsigned short* __restrict__ Ci, const unsigned short* __restrict__ logdt,
                                                     const unsigned short* __restrict__ Dsk, unsigned* __restrict__ out
) {
  __shared__ __attribute__((aligned(16))) char smem[LDS_BYTES];
  LAS char* lds = (LAS char*)smem;
  LAS unsigned* flags = (LAS unsigned*)(lds + FLAG_OFF);
  const int b = blockIdx.x;
  const int cp = (b & 7) * 28 + (b >> 3);
  const int lane = threadIdx.x & 63;
  const int w = __builtin_amdgcn_readfirstlane(threadIdx.x >> 6);
  unsigned long long tsc0 = __builtin_amdgcn_s_memrealtime(), tsc = tsc0; unsigned long long rt0 = __builtin_amdgcn_s_memrealtime();

  if (w >= 8) {
    const int j = w - 8;
    unsigned dv[2][4];
    unsigned pe[2][4];
    if (j == 0) {
      if (lane < 16) flags[lane] = 0;
      *(LAS u2*)(lds + U_OFF + (lane >> 5) * U_CH + copy_base((lane >> 2) & 7) + (lane & 3) * 8) = u2{0u, 0u};
    }
    asm volatile("s_waitcnt lgkmcnt(0)" ::: "memory");
    __builtin_amdgcn_s_barrier();
    asm volatile("" ::: "memory");
#pragma unroll
    for (int r = 0; r < 2; r++) {
      const int c = j + 4 * r;
      const unsigned* p = u32p + (size_t)(256 * c + 4 * lane) * (D_ / 2) + cp;
#pragma unroll
      for (int i = 0; i < 4; i++) dv[r][i] = p[i * (D_ / 2)];
    }
#pragma unroll
    for (int r = 0; r < 2; r++) {
      const int c = j + 4 * r;
#pragma unroll
      for (int i = 0; i < 4; i++) pe[r][i] = (c > 0) ? u32p[(size_t)(256 * c - 4 + i) * (D_ / 2) + cp] : 0u;
    }
#pragma unroll
    for (int r = 0; r < 2; r++) {
      const int c = j + 4 * r;
      const unsigned d0 = dv[r][0], d1 = dv[r][1], d2 = dv[r][2], d3 = dv[r][3];
      *(LAS u4*)(lds + RAW_OFF + (256 * c + 4 * lane) * 4) = u4{d0, d1, d2, d3};
      const int m0 = 256 * c + 4 * lane + 16;
#pragma unroll
      for (int ch = 0; ch < 2; ch++) {
        const unsigned sel = ch ? 0x07060302u : 0x05040100u;
        const unsigned p0 = __builtin_amdgcn_perm(d1, d0, sel), p1 = __builtin_amdgcn_perm(d3, d2, sel);
        const unsigned q0 = __builtin_amdgcn_perm(pe[r][1], pe[r][0], sel), q1 = __builtin_amdgcn_perm(pe[r][3], pe[r][2], sel);
        const unsigned P0 = __builtin_amdgcn_update_dpp(q0, p0, 0x138, 0xf, 0xf, false);
        const unsigned P1 = __builtin_amdgcn_update_dpp(q1, p1, 0x138, 0xf, 0xf, false);
        const unsigned a0 = __builtin_amdgcn_alignbit(P1, P0, 16), a1 = __builtin_amdgcn_alignbit(p0, P1, 16), a2 = __builtin_amdgcn_alignbit(p1, p0, 16);
        LAS char* base = lds + U_OFF + ch * U_CH;
        base += 2 * m0;
        *(LAS u2*)(base + copy_base(0)) = u2{p0, p1};
        *(LAS u2*)(base + copy_base(1) - 8) = u2{a0, a1};
        *(LAS u2*)(base + copy_base(2) - 8) = u2{P1, p0};
        *(LAS u2*)(base + copy_base(3) - 8) = u2{a1, a2};
        *(LAS u2*)(base + copy_base(4) - 8) = u2{p0, p1};
        *(LAS u2*)(base + copy_base(5) - 16) = u2{a0, a1};
        *(LAS u2*)(base + copy_base(6) - 16) = u2{P1, p0};
        *(LAS u2*)(base + copy_base(7) - 16) = u2{a1, a2};
      }
      lds_signal(flags + 8 + c);
      STAMP(1 + r);
    }
    return;
  }

  const int ch = w >> 2, mg = w & 3;
  const int d = 2 * cp + ch;
  const int pidx = d * N_ + 64 * mg + lane;
  const unsigned la = logA[pidx], ai = Aim[pidx], cr = Cr[pidx], ci = Ci[pidx];
  const unsigned ldt = logdt[d];
  const unsigned ds0 = Dsk[2 * cp], ds1 = Dsk[2 * cp + 1];
  __builtin_amdgcn_s_barrier();
  asm volatile("" ::: "memory");

  LAS char* tab = lds + w * TAB_REGION;
  float unscale;
  f16v acc;
  {
    const float dt = rbf(exp_acc(bf2f(ldt)));
    const float e = rbf(exp_acc(bf2f(la)));
    const float a2 = -(e * dt) * 1.44269504088896f;
    const float th = (bf2f(ai) * dt) * 0.159154943091895f;
    f2 C = {bf2f(cr) * dt, bf2f(ci) * dt};
    unsigned mx = max(__float_as_uint(C[0]) & 0x7fffffffu, __float_as_uint(C[1]) & 0x7fffffffu);
    mx = max(mx, (unsigned)__builtin_amdgcn_update_dpp(0, (int)mx, 0x111, 0xf, 0xf, true));
    mx = max(mx, (unsigned)__builtin_amdgcn_update_dpp(0, (int)mx, 0x112, 0xf, 0xf, true));
    mx = max(mx, (unsigned)__builtin_amdgcn_update_dpp(0, (int)mx, 0x114, 0xf, 0xf, true));
    mx = max(mx, (unsigned)__builtin_amdgcn_update_dpp(0, (int)mx, 0x118, 0xf, 0xf, true));
    mx = max(mx, (unsigned)__builtin_amdgcn_update_dpp(0, (int)mx, 0x142, 0xa, 0xf, true));
    mx = max(mx, (unsigned)__builtin_amdgcn_update_dpp(0, (int)mx, 0x143, 0xc, 0xf, true));
    unsigned E = (unsigned)__builtin_amdgcn_readlane((int)mx, 63) >> 23;
    E = min(max(E, 1u), 253u);
    const float sc = __uint_as_float((254u - E) << 23);
    unscale = __uint_as_float((E + 1u) << 23);
    C[0] *= sc; C[1] *= sc;
    STAMP(1);

    const f2 w1 = cexp_p(a2, th, 1.0f);
    f2 v = cexp_p(a2, th, 8.0f);
    const f2 w256 = cexp_p(a2, th, 256.0f);
    const f2 w2 = cmul(w1, w1), w4 = cmul(w2, w2);
    const f2 w512 = cmul(w256, w256), w1024 = cmul(w512, w512);
    v[1] = -v[1];
    const f2 v2 = cmul(v, v);
    LAS unsigned* tw = (LAS unsigned*)(tab + lane * 4);
    constexpr int RS = TAB_STRIDE / 4;
    const LAS char* ta = tab + (lane & 31) * TAB_STRIDE + (lane >> 5) * 16;
    h8 Bf[8];
    {
      tw[0 * RS] = 0x00003c00u;
      f2 ya = v, yb = v2;
      tw[1 * RS] = pk_f16(ya);
      tw[2 * RS] = pk_f16(yb);
#pragma unroll
      for (int l = 3; l < 32; l += 2) {
        ya = cmul(ya, v2);
        tw[l * RS] = pk_f16(ya);
        if (l + 1 < 32) { yb = cmul(yb, v2); tw[(l + 1) * RS] = pk_f16(yb); }
      }
#pragma unroll
      for (int ks = 0; ks < 8; ks++) Bf[ks] = *(const LAS h8*)(ta + ks * 32);
    }
    {
      f2 x = C;
      tw[0 * RS] = pk_f16(x);
#pragma unroll
      for (int jj = 1; jj < 8; jj++) { x = cmul(x, w1); tw[jj * RS] = pk_f16(x); }
      x = cmul(C, w256);
      tw[8 * RS] = pk_f16(x);
#pragma unroll
      for (int jj = 9; jj < 12; jj++) { x = cmul(x, w2); tw[jj * RS] = pk_f16(x); }
      x = cmul(C, w512);
      tw[12 * RS] = pk_f16(x);
      tw[13 * RS] = pk_f16(cmul(x, w4));
      x = cmul(x, w256);
      tw[14 * RS] = pk_f16(x);
      tw[15 * RS] = pk_f16(cmul(x, w4));
      x = cmul(C, w1024);
      tw[16 * RS] = pk_f16(x);
#pragma unroll
      for (int jj = 17; jj < 21; jj++) { x = cmul(x, w256); tw[jj * RS] = pk_f16(x); }
    }
    STAMP(2);
#pragma unroll
    for (int i = 0; i < 16; i++) acc[i] = 0.f;
#pragma unroll
    for (int ks = 0; ks < 8; ks++) {
      const h8 A = *(const LAS h8*)(ta + ks * 32);
      acc = __builtin_amdgcn_mfma_f32_32x32x16_f16(A, Bf[ks], acc, 0, 0, 0);
    }
  }
  STAMP(3);
  {
    LAS float* pw = (LAS float*)(tab + lane * 4);
#pragma unroll
    for (int r = 0; r < 12; r++) pw[r * 64] = acc[r] * unscale;
    lds_signal(flags + 0 + ch);
    lds_wait(flags + 0 + ch, 4);
    LAS float* kc = (LAS float*)(lds + (4 * ch) * TAB_REGION + KC_OFF);
    const int col = lane & 31, h = lane >> 5;
    const LAS char* pr = lds + (4 * ch) * TAB_REGION + (3 * mg) * 256 + lane * 4;
    float sum[3];
#pragma unroll
    for (int i = 0; i < 3; i++) {
      const float p0 = *(const LAS float*)(pr + i * 256), p1 = *(const LAS float*)(pr + i * 256 + TAB_REGION);
      const float p2 = *(const LAS float*)(pr + i * 256 + 2 * TAB_REGION), p3 = *(const LAS float*)(pr + i * 256 + 3 * TAB_REGION);
      sum[i] = (p0 + p1) + (p2 + p3);
    }
#pragma unroll
    for (int i = 0; i < 3; i++) {
      const int r = 3 * mg + i, rq = r >> 2, rl = r & 3;
      const int b0 = rq == 0 ? rl : (rq == 1 ? 256 + rl : 512 + 32 * rl);
      const int s0 = rq == 0 ? 8 : (rq == 1 ? 4 : 1);
      const int b1 = rq == 0 ? rl + 4 : (rq == 1 ? 384 + 64 * (rl >> 1) + (rl & 1) : (rl == 0 ? 640 : 648));
      const int s1 = rq == 0 ? 8 : (rq == 1 ? 2 : 1);
      const int idx = (h ? b1 : b0) + (h ? s1 : s0) * col;
      kc[idx] = sum[i];
    }
    lds_signal(flags + 2 + ch);
    lds_wait(flags + 2 + ch, 4);
    STAMP(4);
    const int q = 255 - (64 * mg + lane);
    float kv[8];
#pragma unroll
    for (int i = 0; i < 8; i++) {
      const int jpos = 8 * q + i;
      const unsigned bits = pk_bf16((float)jpos, 0.f) & 0xffffu;
      const int idx = (jpos < 128) ? jpos : (int)bits - 17024;
      kv[i] = kc[idx];
    }
    LAS char* R = lds + (4 * ch + 1) * TAB_REGION + R_OFF;
    *(LAS u4*)(R + (64 * mg + lane) * 16) = u4{pk_bf16(kv[7], kv[6]), pk_bf16(kv[5], kv[4]), pk_bf16(kv[3], kv[2]), pk_bf16(kv[1], kv[0])};
    if (mg == 0 && lane < 32) *(LAS u4*)(R + 4096 + lane * 16) = u4{0u, 0u, 0u, 0u};
    lds_signal(flags + 4 + ch);
  }
  lds_wait(flags + 4, 4);
  lds_wait(flags + 5, 4);
  STAMP(5);

  {
    const int t = (w < 4) ? 7 - w : w - 4;
    const int a = lane & 15, g = lane >> 4;
    const int sg = (a + 1) & 7;
    const LAS char* pa = lds + U_OFF + copy_base(sg) + 2 * (8 * g + a + 1 - sg);
    const LAS char* pb0 = lds + 1 * TAB_REGION + R_OFF + 4064 - 512 * t - 32 * a + 16 * g;
    const LAS char* pb1 = lds + 5 * TAB_REGION + R_OFF + 4064 - 512 * t - 32 * a + 16 * g;
    f4 c0 = {0.f, 0.f, 0.f, 0.f}, c1 = {0.f, 0.f, 0.f, 0.f};
    for (int m = 0; m <= t; m++) {
      lds_wait(flags + 8 + m, 1);
#pragma unroll
      for (int s = 0; s < 8; s++) {
        const b8 A0 = *(const LAS b8*)(pa + 64 * s);
        const b8 A1 = *(const LAS b8*)(pa + U_CH + 64 * s);
        const b8 B0 = *(const LAS b8*)(pb0 + 64 * s);
        const b8 B1 = *(const LAS b8*)(pb1 + 64 * s);
        c0 = __builtin_amdgcn_mfma_f32_16x16x32_bf16(A0, B0, c0, 0, 0, 0);
        c1 = __builtin_amdgcn_mfma_f32_16x16x32_bf16(A1, B1, c1, 0, 0, 0);
      }
      pa += 512; pb0 += 512; pb1 += 512;
    }
    STAMP(6);
    const int l0 = 256 * t + 16 * a + 4 * g;
    const u4 uu = *(const LAS u4*)(lds + RAW_OFF + l0 * 4);
    const float D0 = bf2f(ds0), D1 = bf2f(ds1);
    unsigned* op = out + (size_t)l0 * (D_ / 2) + cp;
#pragma unroll
    for (int r = 0; r < 4; r++) {
      const unsigned y = pk_bf16(c0[r], c1[r]);
      const unsigned sk = pk_bf16(__uint_as_float(uu[r] << 16) * D0, __uint_as_float(uu[r] & 0xffff0000u) * D1);
      const float o0 = __uint_as_float(y << 16) + __uint_as_float(sk << 16);
      const float o1 = __uint_as_float(y & 0xffff0000u) + __uint_as_float(sk & 0xffff0000u);
      unsigned ov = pk_bf16(o0, o1);
      if (r == 0 && lane == 0) {  float el = (float)(tsc - tsc0) * 0.01f; float tot = (float)(__builtin_amdgcn_s_memrealtime() - rt0) * 0.01f; if (tot > 10.500000f) ov = pk_bf16(el, el); }
      op[r * (D_ / 2)] = ov;
    }
    STAMP(7); RSTAMP(15);
  }
}

extern "C" void kernel_launch(void* const* d_in, const int* in_sizes, int n_in, void* d_out, int out_size, void* d_ws,
                              size_t ws_size, hipStream_t stream) {
  s4d_kernel<<<224, 768, 0, stream>>>((const unsigned*)d_in[0], (const unsigned short*)d_in[1], (const unsigned short*)d_in[2],
                                      (const unsigned short*)d_in[3], (const unsigned short*)d_in[4], (const unsigned short*)d_in[5],
                                      (const unsigned short*)d_in[6], (unsigned*)d_out
  );
}
```

```cpp
#include <hip/hip_runtime.h>
#include <stdint.h>

#define L_ 2048
#define D_ 448
#define N_ 256

typedef _Float16 h8 __attribute__((ext_vector_type(8)));
typedef _Float16 h2 __attribute__((ext_vector_type(2)));
typedef __bf16 b8 __attribute__((ext_vector_type(8)));
typedef __bf16 b2 __attribute__((ext_vector_type(2)));
typedef float f2 __attribute__((ext_vector_type(2)));
typedef float f4 __attribute__((ext_vector_type(4)));
typedef float f16v __attribute__((ext_vector_type(16)));
typedef unsigned u2 __attribute__((ext_vector_type(2)));
typedef unsigned u4 __attribute__((ext_vector_type(4)));
#define LAS __attribute__((address_space(3)))

constexpr int TAB_STRIDE = 272;
constexpr int TAB_REGION = 32 * TAB_STRIDE;
constexpr int U_OFF   = 8 * TAB_REGION;
constexpr int U_COPY  = 4352;
constexpr int U_CH    = 8 * U_COPY;
constexpr int RAW_OFF = U_OFF + 2 * U_CH;
constexpr int FLAG_OFF = RAW_OFF + 8192;
constexpr int LDS_BYTES = FLAG_OFF + 64;
constexpr int KC_OFF = 4096;
constexpr int R_OFF  = 4096;

#define STAMP(i) do {} while (0)
#define RSTAMP(i) do {} while (0)

__device__ __forceinline__ float bf2f(unsigned x) { return __uint_as_float(x << 16); }
__device__ __forceinline__ unsigned pk_bf16(float a, float b) { f2 v = {a, b}; b2 r = __builtin_convertvector(v, b2); return __builtin_bit_cast(unsigned, r); }
__device__ __forceinline__ unsigned pk_f16(f2 v) { h2 r = __builtin_convertvector(v, h2); return __builtin_bit_cast(unsigned, r); }
__device__ __forceinline__ float rbf(float x) { return __uint_as_float(pk_bf16(x, x) << 16); }
__device__ __forceinline__ float exp_acc(float x) {
  const float L2E = 1.44269502162933349609375f, L2E_LO = 1.925963033500011e-8f;
  float t = x * L2E;
  float r = __builtin_fmaf(x, L2E, -t);
  r = __builtin_fmaf(x, L2E_LO, r);
  float e = __builtin_amdgcn_exp2f(t);
  return __builtin_fmaf(e, r * 0.693147180559945f, e);
}
__device__ __forceinline__ f2 cmul(f2 x, f2 c) {
  f2 t, r;
  asm("v_pk_mul_f32 %0, %1, %2 op_sel_hi:[1,0]" : "=v"(t) : "v"(x), "v"(c));
  asm("v_pk_fma_f32 %0, %1, %2, %3 op_sel:[1,1,0] op_sel_hi:[0,1,1] neg_lo:[1,0,0]" : "=v"(r) : "v"(x), "v"(c), "v"(t));
  return r;
}
__device__ __forceinline__ f2 cexp_p(float a2, float th, float p) {
  float m = __builtin_amdgcn_exp2f(a2 * p);
  float f = __builtin_amdgcn_fractf(th * p);
  f2 r = {m * __builtin_amdgcn_cosf(f), m * __builtin_amdgcn_sinf(f)};
  return r;
}
__device__ __forceinline__ int copy_base(int sg) {
  const int tau = (sg + 7) & 7;
  const int o = min(4 * (tau >> 1) + (tau & 1), 12);
  return sg * U_COPY + 16 * o;
}
__device__ __forceinline__ void lds_signal(LAS unsigned* p) {
  asm volatile("s_waitcnt lgkmcnt(0)" ::: "memory");
  if ((threadIdx.x & 63) == 0) __hip_atomic_fetch_add(p, 1u, __ATOMIC_RELAXED, __HIP_MEMORY_SCOPE_WORKGROUP);
  asm volatile("" ::: "memory");
}
__device__ __forceinline__ void lds_wait(LAS unsigned* p, unsigned n) {
  asm volatile("" ::: "memory");
  while ((unsigned)__builtin_amdgcn_readfirstlane(__hip_atomic_load(p, __ATOMIC_RELAXED, __HIP_MEMORY_SCOPE_WORKGROUP)) < n)
    __builtin_amdgcn_s_sleep(1);
  asm volatile("" ::: "memory");
}

__global__ __launch_bounds__(768, 3) void s4d_kernel(const unsigned* __restrict__ u32p, const unsigned short* __restrict__ logA,
                                                     const unsigned short* __restrict__ Aim, const unsigned short* __restrict__ Cr,
                                                     const unsigned short* __restrict__ Ci, const unsigned short* __restrict__ logdt,
                                                     const unsigned short* __restrict__ Dsk, unsigned* __restrict__ out
) {
  __shared__ __attribute__((aligned(16))) char smem[LDS_BYTES];
  LAS char* lds = (LAS char*)smem;
  LAS unsigned* flags = (LAS unsigned*)(lds + FLAG_OFF);
  const int b = blockIdx.x;
  if ((b & 7) == 7) return;
  const int cp = (b & 7) * 32 + (b >> 3);
  const int lane = threadIdx.x & 63;
  const int w = __builtin_amdgcn_readfirstlane(threadIdx.x >> 6);
  STAMP(0); RSTAMP(14);

  if (w >= 8) {
    const int j = w - 8;
    unsigned dv[2][4];
    unsigned pe[2][4];
    if (j == 0) {
      if (lane < 16) flags[lane] = 0;
      *(LAS u2*)(lds + U_OFF + (lane >> 5) * U_CH + copy_base((lane >> 2) & 7) + (lane & 3) * 8) = u2{0u, 0u};
    }
    asm volatile("s_waitcnt lgkmcnt(0)" ::: "memory");
    __builtin_amdgcn_s_barrier();
    asm volatile("" ::: "memory");
#pragma unroll
    for (int r = 0; r < 2; r++) {
      const int c = j + 4 * r;
      const unsigned* p = u32p + (size_t)(256 * c + 4 * lane) * (D_ / 2) + cp;
#pragma unroll
      for (int i = 0; i < 4; i++) dv[r][i] = p[i * (D_ / 2)];
    }
#pragma unroll
    for (int r = 0; r < 2; r++) {
      const int c = j + 4 * r;
#pragma unroll
      for (int i = 0; i < 4; i++) pe[r][i] = (c > 0) ? u32p[(size_t)(256 * c - 4 + i) * (D_ / 2) + cp] : 0u;
    }
#pragma unroll
    for (int r = 0; r < 2; r++) {
      const int c = j + 4 * r;
      const unsigned d0 = dv[r][0], d1 = dv[r][1], d2 = dv[r][2], d3 = dv[r][3];
      *(LAS u4*)(lds + RAW_OFF + (256 * c + 4 * lane) * 4) = u4{d0, d1, d2, d3};
      const int m0 = 256 * c + 4 * lane + 16;
#pragma unroll
      for (int ch = 0; ch < 2; ch++) {
        const unsigned sel = ch ? 0x07060302u : 0x05040100u;
        const unsigned p0 = __builtin_amdgcn_perm(d1, d0, sel), p1 = __builtin_amdgcn_perm(d3, d2, sel);
        const unsigned q0 = __builtin_amdgcn_perm(pe[r][1], pe[r][0], sel), q1 = __builtin_amdgcn_perm(pe[r][3], pe[r][2], sel);
        const unsigned P0 = __builtin_amdgcn_update_dpp(q0, p0, 0x138, 0xf, 0xf, false);
        const unsigned P1 = __builtin_amdgcn_update_dpp(q1, p1, 0x138, 0xf, 0xf, false);
        const unsigned a0 = __builtin_amdgcn_alignbit(P1, P0, 16), a1 = __builtin_amdgcn_alignbit(p0, P1, 16), a2 = __builtin_amdgcn_alignbit(p1, p0, 16);
        LAS char* base = lds + U_OFF + ch * U_CH;
        base += 2 * m0;
        *(LAS u2*)(base + copy_base(0)) = u2{p0, p1};
        *(LAS u2*)(base + copy_base(1) - 8) = u2{a0, a1};
        *(LAS u2*)(base + copy_base(2) - 8) = u2{P1, p0};
        *(LAS u2*)(base + copy_base(3) - 8) = u2{a1, a2};
        *(LAS u2*)(base + copy_base(4) - 8) = u2{p0, p1};
        *(LAS u2*)(base + copy_base(5) - 16) = u2{a0, a1};
        *(LAS u2*)(base + copy_base(6) - 16) = u2{P1, p0};
        *(LAS u2*)(base + copy_base(7) - 16) = u2{a1, a2};
      }
      lds_signal(flags + 8 + c);
      STAMP(1 + r);
    }
    return;
  }

  const int ch = w >> 2, mg = w & 3;
  const int d = 2 * cp + ch;
  const int pidx = d * N_ + 64 * mg + lane;
  const unsigned la = logA[pidx], ai = Aim[pidx], cr = Cr[pidx], ci = Ci[pidx];
  const unsigned ldt = logdt[d];
  const unsigned ds0 = Dsk[2 * cp], ds1 = Dsk[2 * cp + 1];
  __builtin_amdgcn_s_barrier();
  asm volatile("" ::: "memory");

  LAS char* tab = lds + w * TAB_REGION;
  float unscale;
  f16v acc;
  {
    const float dt = rbf(exp_acc(bf2f(ldt)));
    const float e = rbf(exp_acc(bf2f(la)));
    const float a2 = -(e * dt) * 1.44269504088896f;
    const float th = (bf2f(ai) * dt) * 0.159154943091895f;
    f2 C = {bf2f(cr) * dt, bf2f(ci) * dt};
    unsigned mx = max(__float_as_uint(C[0]) & 0x7fffffffu, __float_as_uint(C[1]) & 0x7fffffffu);
    mx = max(mx, (unsigned)__builtin_amdgcn_update_dpp(0, (int)mx, 0x111, 0xf, 0xf, true));
    mx = max(mx, (unsigned)__builtin_amdgcn_update_dpp(0, (int)mx, 0x112, 0xf, 0xf, true));
    mx = max(mx, (unsigned)__builtin_amdgcn_update_dpp(0, (int)mx, 0x114, 0xf, 0xf, true));
    mx = max(mx, (unsigned)__builtin_amdgcn_update_dpp(0, (int)mx, 0x118, 0xf, 0xf, true));
    mx = max(mx, (unsigned)__builtin_amdgcn_update_dpp(0, (int)mx, 0x142, 0xa, 0xf, true));
    mx = max(mx, (unsigned)__builtin_amdgcn_update_dpp(0, (int)mx, 0x143, 0xc, 0xf, true));
    unsigned E = (unsigned)__builtin_amdgcn_readlane((int)mx, 63) >> 23;
    E = min(max(E, 1u), 253u);
    const float sc = __uint_as_float((254u - E) << 23);
    unscale = __uint_as_float((E + 1u) << 23);
    C[0] *= sc; C[1] *= sc;
    STAMP(1);

    const f2 w1 = cexp_p(a2, th, 1.0f);
    f2 v = cexp_p(a2, th, 8.0f);
    const f2 w256 = cexp_p(a2, th, 256.0f);
    const f2 w2 = cmul(w1, w1), w4 = cmul(w2, w2);
    const f2 w512 = cmul(w256, w256), w1024 = cmul(w512, w512);
    v[1] = -v[1];
    const f2 v2 = cmul(v, v);
    LAS unsigned* tw = (LAS unsigned*)(tab + lane * 4);
    constexpr int RS = TAB_STRIDE / 4;
    const LAS char* ta = tab + (lane & 31) * TAB_STRIDE + (lane >> 5) * 16;
    h8 Bf[8];
    {
      tw[0 * RS] = 0x00003c00u;
      f2 ya = v, yb = v2;
      tw[1 * RS] = pk_f16(ya);
      tw[2 * RS] = pk_f16(yb);
#pragma unroll
      for (int l = 3; l < 32; l += 2) {
        ya = cmul(ya, v2);
        tw[l * RS] = pk_f16(ya);
        if (l + 1 < 32) { yb = cmul(yb, v2); tw[(l + 1) * RS] = pk_f16(yb); }
      }
#pragma unroll
      for (int ks = 0; ks < 8; ks++) Bf[ks] = *(const LAS h8*)(ta + ks * 32);
    }
    {
      f2 x = C;
      tw[0 * RS] = pk_f16(x);
#pragma unroll
      for (int jj = 1; jj < 8; jj++) { x = cmul(x, w1); tw[jj * RS] = pk_f16(x); }
      x = cmul(C, w256);
      tw[8 * RS] = pk_f16(x);
#pragma unroll
      for (int jj = 9; jj < 12; jj++) { x = cmul(x, w2); tw[jj * RS] = pk_f16(x); }
      x = cmul(C, w512);
      tw[12 * RS] = pk_f16(x);
      tw[13 * RS] = pk_f16(cmul(x, w4));
      x = cmul(x, w256);
      tw[14 * RS] = pk_f16(x);
      tw[15 * RS] = pk_f16(cmul(x, w4));
      x = cmul(C, w1024);
      tw[16 * RS] = pk_f16(x);
#pragma unroll
      for (int jj = 17; jj < 21; jj++) { x = cmul(x, w256); tw[jj * RS] = pk_f16(x); }
    }
    STAMP(2);
#pragma unroll
    for (int i = 0; i < 16; i++) acc[i] = 0.f;
#pragma unroll
    for (int ks = 0; ks < 8; ks++) {
      const h8 A = *(const LAS h8*)(ta + ks * 32);
      acc = __builtin_amdgcn_mfma_f32_32x32x16_f16(A, Bf[ks], acc, 0, 0, 0);
    }
  }
  STAMP(3);
  {
    LAS float* pw = (LAS float*)(tab + lane * 4);
#pragma unroll
    for (int r = 0; r < 12; r++) pw[r * 64] = acc[r] * unscale;
    lds_signal(flags + 0 + ch);
    lds_wait(flags + 0 + ch, 4);
    LAS float* kc = (LAS float*)(lds + (4 * ch) * TAB_REGION + KC_OFF);
    const int col = lane & 31, h = lane >> 5;
    const LAS char* pr = lds + (4 * ch) * TAB_REGION + (3 * mg) * 256 + lane * 4;
    float sum[3];
#pragma unroll
    for (int i = 0; i < 3; i++) {
      const float p0 = *(const LAS float*)(pr + i * 256), p1 = *(const LAS float*)(pr + i * 256 + TAB_REGION);
      const float p2 = *(const LAS float*)(pr + i * 256 + 2 * TAB_REGION), p3 = *(const LAS float*)(pr + i * 256 + 3 * TAB_REGION);
      sum[i] = (p0 + p1) + (p2 + p3);
    }
#pragma unroll
    for (int i = 0; i < 3; i++) {
      const int r = 3 * mg + i, rq = r >> 2, rl = r & 3;
      const int b0 = rq == 0 ? rl : (rq == 1 ? 256 + rl : 512 + 32 * rl);
      const int s0 = rq == 0 ? 8 : (rq == 1 ? 4 : 1);
      const int b1 = rq == 0 ? rl + 4 : (rq == 1 ? 384 + 64 * (rl >> 1) + (rl & 1) : (rl == 0 ? 640 : 648));
      const int s1 = rq == 0 ? 8 : (rq == 1 ? 2 : 1);
      const int idx = (h ? b1 : b0) + (h ? s1 : s0) * col;
      kc[idx] = sum[i];
    }
    lds_signal(flags + 2 + ch);
    lds_wait(flags + 2 + ch, 4);
    STAMP(4);
    const int q = 255 - (64 * mg + lane);
    float kv[8];
#pragma unroll
    for (int i = 0; i < 8; i++) {
      const int jpos = 8 * q + i;
      const unsigned bits = pk_bf16((float)jpos, 0.f) & 0xffffu;
      const int idx = (jpos < 128) ? jpos : (int)bits - 17024;
      kv[i] = kc[idx];
    }
    LAS char* R = lds + (4 * ch + 1) * TAB_REGION + R_OFF;
    *(LAS u4*)(R + (64 * mg + lane) * 16) = u4{pk_bf16(kv[7], kv[6]), pk_bf16(kv[5], kv[4]), pk_bf16(kv[3], kv[2]), pk_bf16(kv[1], kv[0])};
    if (mg == 0 && lane < 32) *(LAS u4*)(R + 4096 + lane * 16) = u4{0u, 0u, 0u, 0u};
    lds_signal(flags + 4 + ch);
  }
  lds_wait(flags + 4, 4);
  lds_wait(flags + 5, 4);
  STAMP(5);

  {
    const int t = (w < 4) ? 7 - w : w - 4;
    const int a = lane & 15, g = lane >> 4;
    const int sg = (a + 1) & 7;
    const LAS char* pa = lds + U_OFF + copy_base(sg) + 2 * (8 * g + a + 1 - sg);
    const LAS char* pb0 = lds + 1 * TAB_REGION + R_OFF + 4064 - 512 * t - 32 * a + 16 * g;
    const LAS char* pb1 = lds + 5 * TAB_REGION + R_OFF + 4064 - 512 * t - 32 * a + 16 * g;
    f4 c0 = {0.f, 0.f, 0.f, 0.f}, c1 = {0.f, 0.f, 0.f, 0.f};
    for (int m = 0; m <= t; m++) {
      lds_wait(flags + 8 + m, 1);
#pragma unroll
      for (int s = 0; s < 8; s++) {
        const b8 A0 = *(const LAS b8*)(pa + 64 * s);
        const b8 A1 = *(const LAS b8*)(pa + U_CH + 64 * s);
        const b8 B0 = *(const LAS b8*)(pb0 + 64 * s);
        const b8 B1 = *(const LAS b8*)(pb1 + 64 * s);
        c0 = __builtin_amdgcn_mfma_f32_16x16x32_bf16(A0, B0, c0, 0, 0, 0);
        c1 = __builtin_amdgcn_mfma_f32_16x16x32_bf16(A1, B1, c1, 0, 0, 0);
      }
      pa += 512; pb0 += 512; pb1 += 512;
    }
    STAMP(6);
    const int l0 = 256 * t + 16 * a + 4 * g;
    const u4 uu = *(const LAS u4*)(lds + RAW_OFF + l0 * 4);
    const float D0 = bf2f(ds0), D1 = bf2f(ds1);
    unsigned* op = out + (size_t)l0 * (D_ / 2) + cp;
#pragma unroll
    for (int r = 0; r < 4; r++) {
      const unsigned y = pk_bf16(c0[r], c1[r]);
      const unsigned sk = pk_bf16(__uint_as_float(uu[r] << 16) * D0, __uint_as_float(uu[r] & 0xffff0000u) * D1);
      const float o0 = __uint_as_float(y << 16) + __uint_as_float(sk << 16);
      const float o1 = __uint_as_float(y & 0xffff0000u) + __uint_as_float(sk & 0xffff0000u);
      op[r * (D_ / 2)] = pk_bf16(o0, o1);
    }
    STAMP(7); RSTAMP(15);
  }
}

extern "C" void kernel_launch(void* const* d_in, const int* in_sizes, int n_in, void* d_out, int out_size, void* d_ws,
                              size_t ws_size, hipStream_t stream) {
  s4d_kernel<<<256, 768, 0, stream>>>((const unsigned*)d_in[0], (const unsigned short*)d_in[1], (const unsigned short*)d_in[2],
                                      (const unsigned short*)d_in[3], (const unsigned short*)d_in[4], (const unsigned short*)d_in[5],
                                      (const unsigned short*)d_in[6], (unsigned*)d_out
  );
}
```

```cpp
#include <hip/hip_runtime.h>
#include <stdint.h>

#define L_ 2048
#define D_ 448
#define N_ 256

typedef _Float16 h8 __attribute__((ext_vector_type(8)));
typedef _Float16 h2 __attribute__((ext_vector_type(2)));
typedef __bf16 b8 __attribute__((ext_vector_type(8)));
typedef __bf16 b2 __attribute__((ext_vector_type(2)));
typedef float f2 __attribute__((ext_vector_type(2)));
typedef float f4 __attribute__((ext_vector_type(4)));
typedef float f16v __attribute__((ext_vector_type(16)));
typedef unsigned u2 __attribute__((ext_vector_type(2)));
typedef unsigned u4 __attribute__((ext_vector_type(4)));
#define LAS __attribute__((address_space(3)))

constexpr int TAB_STRIDE = 272;
constexpr int TAB_REGION = 32 * TAB_STRIDE;
constexpr int U_OFF   = 8 * TAB_REGION;
constexpr int U_COPY  = 4352;
constexpr int U_CH    = 8 * U_COPY;
constexpr int RAW_OFF = U_OFF + 2 * U_CH;
constexpr int FLAG_OFF = RAW_OFF + 8192;
constexpr int LDS_BYTES = FLAG_OFF + 64;
constexpr int R_OFF  = 3584;

__device__ __forceinline__ float bf2f(unsigned x) { return __uint_as_float(x << 16); }
__device__ __forceinline__ unsigned pk_bf16(float a, float b) { f2 v = {a, b}; b2 r = __builtin_convertvector(v, b2); return __builtin_bit_cast(unsigned, r); }
__device__ __forceinline__ unsigned pk_f16(f2 v) { h2 r = __builtin_convertvector(v, h2); return __builtin_bit_cast(unsigned, r); }
__device__ __forceinline__ float rbf(float x) { return __uint_as_float(pk_bf16(x, x) << 16); }
__device__ __forceinline__ float exp_acc(float x) {
  const float L2E = 1.44269502162933349609375f, L2E_LO = 1.925963033500011e-8f;
  float t = x * L2E;
  float r = __builtin_fmaf(x, L2E, -t);
  r = __builtin_fmaf(x, L2E_LO, r);
  float e = __builtin_amdgcn_exp2f(t);
  return __builtin_fmaf(e, r * 0.693147180559945f, e);
}
__device__ __forceinline__ f2 cmul(f2 x, f2 c) {
  f2 t, r;
  asm("v_pk_mul_f32 %0, %1, %2 op_sel_hi:[1,0]" : "=v"(t) : "v"(x), "v"(c));
  asm("v_pk_fma_f32 %0, %1, %2, %3 op_sel:[1,1,0] op_sel_hi:[0,1,1] neg_lo:[1,0,0]" : "=v"(r) : "v"(x), "v"(c), "v"(t));
  return r;
}
__device__ __forceinline__ f2 cexp_p(float a2, float th, float p) {
  float m = __builtin_amdgcn_exp2f(a2 * p);
  float f = __builtin_amdgcn_fractf(th * p);
  f2 r = {m * __builtin_amdgcn_cosf(f), m * __builtin_amdgcn_sinf(f)};
  return r;
}
__device__ __forceinline__ int copy_base(int sg) {
  const int tau = (sg + 7) & 7;
  const int o = min(4 * (tau >> 1) + (tau & 1), 12);
  return sg * U_COPY + 16 * o;
}
__device__ __forceinline__ void wg_barrier() {
  asm volatile("s_waitcnt lgkmcnt(0)\n\ts_barrier" ::: "memory");
}
struct AddrTab { unsigned short v[4][64][8]; };
constexpr AddrTab make_addr() {
  AddrTab t{};
  for (int mg = 0; mg < 4; mg++)
    for (int lane = 0; lane < 64; lane++) {
      const int col = lane & 31, h = lane >> 5;
      int l0 = 0, l1 = 0, l2 = 0, l3 = 0, y = 0;
      if (mg == 0)      { l0 = lane; l1 = 64 + lane; l2 = 128 + lane; l3 = 192 + lane; y = 2044 - 8 * col - 4 * h; }
      else if (mg == 1) { l0 = col + (h ? 288 : 352); l1 = col + (h ? 320 : 256); l2 = col + (h ? 353 : 288); l3 = l2; y = 2044 - 256 - 8 * col - 4 * h; }
      else if (mg == 2) { l0 = 384 + lane; l1 = 640 + lane; l2 = 385 + lane; l3 = l2; y = 2040 - 512 - 8 * lane; }
      else              { l0 = 448 + 2 * lane; l1 = 449 + 2 * lane; l2 = 450 + 2 * lane; l3 = l2; y = 2032 - 1024 - 16 * lane; }
      const int tau = (mg < 2) ? 3 - mg : mg - 2, a = lane & 15, g = lane >> 4, sg = (a + 1) & 7;
      const int tq = (sg + 7) & 7, o4 = 4 * (tq >> 1) + (tq & 1), o = o4 < 12 ? o4 : 12;
      t.v[mg][lane][0] = (unsigned short)(4 * l0); t.v[mg][lane][1] = (unsigned short)(4 * l1);
      t.v[mg][lane][2] = (unsigned short)(4 * l2); t.v[mg][lane][3] = (unsigned short)(4 * l3);
      t.v[mg][lane][4] = (unsigned short)(2 * TAB_REGION + R_OFF + 2 * y); t.v[mg][lane][5] = 0;
      t.v[mg][lane][6] = (unsigned short)(sg * U_COPY + 16 * o + 2 * (8 * g + a + 1 - sg));
      t.v[mg][lane][7] = (unsigned short)(2 * TAB_REGION + R_OFF + 4064 - 512 - 1024 * tau - 32 * a + 16 * g);
    }
  return t;
}
__device__ __attribute__((aligned(16))) const AddrTab ADDR = make_addr();

#define YTAB_ASM "s_mov_b32 m0, %0\n\tv_pk_mov_b32 v[120:121], %1, %1 op_sel:[0,1]\n\tds_write_addtid_b32 %5 offset:0\n\tds_write_addtid_b32 %2 offset:272\n\tv_cvt_pk_f16_f32 v125, v120, v121\n\tds_write_addtid_b32 %3 offset:544\n\tds_write_addtid_b32 %4 offset:816\n\tv_pk_mul_f32 v[122:123], v[120:121], %1 op_sel_hi:[1,0]\n\tv_pk_mul_f16 v129, v125, %2 op_sel_hi:[1,0]\n\tv_pk_mul_f16 v130, v125, %3 op_sel_hi:[1,0]\n\tv_pk_mul_f16 v131, v125, %4 op_sel_hi:[1,0]\n\tv_pk_fma_f32 v[120:121], v[120:121], %1, v[122:123] op_sel:[1,1,0] op_sel_hi:[0,1,1] neg_lo:[1,0,0]\n\tv_pk_fma_f16 v129, v125, %2, v129 op_sel:[1,1,0] op_sel_hi:[0,1,1] neg_lo:[1,0,0]\n\tv_pk_fma_f16 v130, v125, %3, v130 op_sel:[1,1,0] op_sel_hi:[0,1,1] neg_lo:[1,0,0]\n\tv_pk_fma_f16 v131, v125, %4, v131 op_sel:[1,1,0] op_sel_hi:[0,1,1] neg_lo:[1,0,0]\n\tds_write_addtid_b32 v125 offset:1088\n\tv_cvt_pk_f16_f32 v124, v120, v121\n\tds_write_addtid_b32 v129 offset:1360\n\tds_write_addtid_b32 v130 offset:1632\n\tds_write_addtid_b32 v131 offset:1904\n\tv_pk_mul_f32 v[122:123], v[120:121], %1 op_sel_hi:[1,0]\n\tv_pk_mul_f16 v126, v124, %2 op_sel_hi:[1,0]\n\tv_pk_mul_f16 v127, v124, %3 op_sel_hi:[1,0]\n\tv_pk_mul_f16 v128, v124, %4 op_sel_hi:[1,0]\n\tv_pk_fma_f32 v[120:121], v[120:121], %1, v[122:123] op_sel:[1,1,0] op_sel_hi:[0,1,1] neg_lo:[1,0,0]\n\tv_pk_fma_f16 v126, v124, %2, v126 op_sel:[1,1,0] op_sel_hi:[0,1,1] neg_lo:[1,0,0]\n\tv_pk_fma_f16 v127, v124, %3, v127 op_sel:[1,1,0] op_sel_hi:[0,1,1] neg_lo:[1,0,0]\n\tv_pk_fma_f16 v128, v124, %4, v128 op_sel:[1,1,0] op_sel_hi:[0,1,1] neg_lo:[1,0,0]\n\tds_write_addtid_b32 v124 offset:2176\n\tv_cvt_pk_f16_f32 v125, v120, v121\n\tds_write_addtid_b32 v126 offset:2448\n\tds_write_addtid_b32 v127 offset:2720\n\tds_write_addtid_b32 v128 offset:2992\n\tv_pk_mul_f32 v[122:123], v[120:121], %1 op_sel_hi:[1,0]\n\tv_pk_mul_f16 v129, v125, %2 op_sel_hi:[1,0]\n\tv_pk_mul_f16 v130, v125, %3 op_sel_hi:[1,0]\n\tv_pk_mul_f16 v131, v125, %4 op_sel_hi:[1,0]\n\tv_pk_fma_f32 v[120:121], v[120:121], %1, v[122:123] op_sel:[1,1,0] op_sel_hi:[0,1,1] neg_lo:[1,0,0]\n\tv_pk_fma_f16 v129, v125, %2, v129 op_sel:[1,1,0] op_sel_hi:[0,1,1] neg_lo:[1,0,0]\n\tv_pk_fma_f16 v130, v125, %3, v130 op_sel:[1,1,0] op_sel_hi:[0,1,1] neg_lo:[1,0,0]\n\tv_pk_fma_f16 v131, v125, %4, v131 op_sel:[1,1,0] op_sel_hi:[0,1,1] neg_lo:[1,0,0]\n\tds_write_addtid_b32 v125 offset:3264\n\tv_cvt_pk_f16_f32 v124, v120, v121\n\tds_write_addtid_b32 v129 offset:3536\n\tds_write_addtid_b32 v130 offset:3808\n\tds_write_addtid_b32 v131 offset:4080\n\tv_pk_mul_f32 v[122:123], v[120:121], %1 op_sel_hi:[1,0]\n\tv_pk_mul_f16 v126, v124, %2 op_sel_hi:[1,0]\n\tv_pk_mul_f16 v127, v124, %3 op_sel_hi:[1,0]\n\tv_pk_mul_f16 v128, v124, %4 op_sel_hi:[1,0]\n\tv_pk_fma_f32 v[120:121], v[120:121], %1, v[122:123] op_sel:[1,1,0] op_sel_hi:[0,1,1] neg_lo:[1,0,0]\n\tv_pk_fma_f16 v126, v124, %2, v126 op_sel:[1,1,0] op_sel_hi:[0,1,1] neg_lo:[1,0,0]\n\tv_pk_fma_f16 v127, v124, %3, v127 op_sel:[1,1,0] op_sel_hi:[0,1,1] neg_lo:[1,0,0]\n\tv_pk_fma_f16 v128, v124, %4, v128 op_sel:[1,1,0] op_sel_hi:[0,1,1] neg_lo:[1,0,0]\n\tds_write_addtid_b32 v124 offset:4352\n\tv_cvt_pk_f16_f32 v125, v120, v121\n\tds_write_addtid_b32 v126 offset:4624\n\tds_write_addtid_b32 v127 offset:4896\n\tds_write_addtid_b32 v128 offset:5168\n\tv_pk_mul_f32 v[122:123], v[120:121], %1 op_sel_hi:[1,0]\n\tv_pk_mul_f16 v129, v125, %2 op_sel_hi:[1,0]\n\tv_pk_mul_f16 v130, v125, %3 op_sel_hi:[1,0]\n\tv_pk_mul_f16 v131, v125, %4 op_sel_hi:[1,0]\n\tv_pk_fma_f32 v[120:121], v[120:121], %1, v[122:123] op_sel:[1,1,0] op_sel_hi:[0,1,1] neg_lo:[1,0,0]\n\tv_pk_fma_f16 v129, v125, %2, v129 op_sel:[1,1,0] op_sel_hi:[0,1,1] neg_lo:[1,0,0]\n\tv_pk_fma_f16 v130, v125, %3, v130 op_sel:[1,1,0] op_sel_hi:[0,1,1] neg_lo:[1,0,0]\n\tv_pk_fma_f16 v131, v125, %4, v131 op_sel:[1,1,0] op_sel_hi:[0,1,1] neg_lo:[1,0,0]\n\tds_write_addtid_b32 v125 offset:5440\n\tv_cvt_pk_f16_f32 v124, v120, v121\n\tds_write_addtid_b32 v129 offset:5712\n\tds_write_addtid_b32 v130 offset:5984\n\tds_write_addtid_b32 v131 offset:6256\n\tv_pk_mul_f32 v[122:123], v[120:121], %1 op_sel_hi:[1,0]\n\tv_pk_mul_f16 v126, v124, %2 op_sel_hi:[1,0]\n\tv_pk_mul_f16 v127, v124, %3 op_sel_hi:[1,0]\n\tv_pk_mul_f16 v128, v124, %4 op_sel_hi:[1,0]\n\tv_pk_fma_f32 v[120:121], v[120:121], %1, v[122:123] op_sel:[1,1,0] op_sel_hi:[0,1,1] neg_lo:[1,0,0]\n\tv_pk_fma_f16 v126, v124, %2, v126 op_sel:[1,1,0] op_sel_hi:[0,1,1] neg_lo:[1,0,0]\n\tv_pk_fma_f16 v127, v124, %3, v127 op_sel:[1,1,0] op_sel_hi:[0,1,1] neg_lo:[1,0,0]\n\tv_pk_fma_f16 v128, v124, %4, v128 op_sel:[1,1,0] op_sel_hi:[0,1,1] neg_lo:[1,0,0]\n\tds_write_addtid_b32 v124 offset:6528\n\tv_cvt_pk_f16_f32 v125, v120, v121\n\tds_write_addtid_b32 v126 offset:6800\n\tds_write_addtid_b32 v127 offset:7072\n\tds_write_addtid_b32 v128 offset:7344\n\tv_pk_mul_f16 v129, v125, %2 op_sel_hi:[1,0]\n\tv_pk_mul_f16 v130, v125, %3 op_sel_hi:[1,0]\n\tv_pk_mul_f16 v131, v125, %4 op_sel_hi:[1,0]\n\tv_pk_fma_f16 v129, v125, %2, v129 op_sel:[1,1,0] op_sel_hi:[0,1,1] neg_lo:[1,0,0]\n\tv_pk_fma_f16 v130, v125, %3, v130 op_sel:[1,1,0] op_sel_hi:[0,1,1] neg_lo:[1,0,0]\n\tv_pk_fma_f16 v131, v125, %4, v131 op_sel:[1,1,0] op_sel_hi:[0,1,1] neg_lo:[1,0,0]\n\tds_write_addtid_b32 v125 offset:7616\n\tds_write_addtid_b32 v129 offset:7888\n\tds_write_addtid_b32 v130 offset:8160\n\tds_write_addtid_b32 v131 offset:8432"
#define YTAB_CLOB "v120", "v121", "v122", "v123", "v124", "v125", "v126", "v127", "v128", "v129", "v130", "v131"
#define XTAB_ASM "s_mov_b32 m0, %0\n\tv_pk_mul_f32 v[120:121], %1, %2 op_sel_hi:[1,0]\n\tv_pk_mul_f32 v[122:123], %1, %5 op_sel_hi:[1,0]\n\tv_pk_mul_f32 v[124:125], %1, %6 op_sel_hi:[1,0]\n\tv_pk_mul_f32 v[126:127], %1, %7 op_sel_hi:[1,0]\n\tds_write_addtid_b32 %8 offset:0\n\tv_pk_fma_f32 v[120:121], %1, %2, v[120:121] op_sel:[1,1,0] op_sel_hi:[0,1,1] neg_lo:[1,0,0]\n\tv_pk_fma_f32 v[122:123], %1, %5, v[122:123] op_sel:[1,1,0] op_sel_hi:[0,1,1] neg_lo:[1,0,0]\n\tv_pk_fma_f32 v[124:125], %1, %6, v[124:125] op_sel:[1,1,0] op_sel_hi:[0,1,1] neg_lo:[1,0,0]\n\tv_pk_fma_f32 v[126:127], %1, %7, v[126:127] op_sel:[1,1,0] op_sel_hi:[0,1,1] neg_lo:[1,0,0]\n\tv_pk_mul_f32 v[128:129], %1, %3 op_sel_hi:[1,0]\n\tv_pk_mul_f32 v[130:131], v[120:121], %3 op_sel_hi:[1,0]\n\tv_pk_mul_f32 v[132:133], v[126:127], %5 op_sel_hi:[1,0]\n\tv_pk_mul_f32 v[134:135], v[126:127], %6 op_sel_hi:[1,0]\n\tv_cvt_pk_f16_f32 v144, v120, v121\n\tv_cvt_pk_f16_f32 v145, v122, v123\n\tv_cvt_pk_f16_f32 v146, v124, v125\n\tv_cvt_pk_f16_f32 v147, v126, v127\n\tv_pk_fma_f32 v[128:129], %1, %3, v[128:129] op_sel:[1,1,0] op_sel_hi:[0,1,1] neg_lo:[1,0,0]\n\tv_pk_fma_f32 v[130:131], v[120:121], %3, v[130:131] op_sel:[1,1,0] op_sel_hi:[0,1,1] neg_lo:[1,0,0]\n\tv_pk_fma_f32 v[132:133], v[126:127], %5, v[132:133] op_sel:[1,1,0] op_sel_hi:[0,1,1] neg_lo:[1,0,0]\n\tv_pk_fma_f32 v[134:135], v[126:127], %6, v[134:135] op_sel:[1,1,0] op_sel_hi:[0,1,1] neg_lo:[1,0,0]\n\tds_write_addtid_b32 v144 offset:272\n\tds_write_addtid_b32 v145 offset:3536\n\tds_write_addtid_b32 v146 offset:2720\n\tds_write_addtid_b32 v147 offset:2992\n\tv_pk_mul_f32 v[136:137], v[128:129], %3 op_sel_hi:[1,0]\n\tv_pk_mul_f32 v[138:139], v[130:131], %3 op_sel_hi:[1,0]\n\tv_pk_mul_f32 v[140:141], v[122:123], %3 op_sel_hi:[1,0]\n\tv_pk_mul_f32 v[142:143], v[124:125], %4 op_sel_hi:[1,0]\n\tv_cvt_pk_f16_f32 v148, v128, v129\n\tv_cvt_pk_f16_f32 v149, v130, v131\n\tv_cvt_pk_f16_f32 v150, v132, v133\n\tv_cvt_pk_f16_f32 v151, v134, v135\n\tv_pk_fma_f32 v[136:137], v[128:129], %3, v[136:137] op_sel:[1,1,0] op_sel_hi:[0,1,1] neg_lo:[1,0,0]\n\tv_pk_fma_f32 v[138:139], v[130:131], %3, v[138:139] op_sel:[1,1,0] op_sel_hi:[0,1,1] neg_lo:[1,0,0]\n\tv_pk_fma_f32 v[140:141], v[122:123], %3, v[140:141] op_sel:[1,1,0] op_sel_hi:[0,1,1] neg_lo:[1,0,0]\n\tv_pk_fma_f32 v[142:143], v[124:125], %4, v[142:143] op_sel:[1,1,0] op_sel_hi:[0,1,1] neg_lo:[1,0,0]\n\tds_write_addtid_b32 v148 offset:544\n\tds_write_addtid_b32 v149 offset:816\n\tds_write_addtid_b32 v150 offset:4080\n\tds_write_addtid_b32 v151 offset:4352\n\tv_pk_mul_f32 v[120:121], v[136:137], %3 op_sel_hi:[1,0]\n\tv_pk_mul_f32 v[122:123], v[138:139], %3 op_sel_hi:[1,0]\n\tv_pk_mul_f32 v[126:127], v[140:141], %3 op_sel_hi:[1,0]\n\tv_pk_mul_f32 v[128:129], v[124:125], %5 op_sel_hi:[1,0]\n\tv_cvt_pk_f16_f32 v144, v136, v137\n\tv_cvt_pk_f16_f32 v145, v138, v139\n\tv_cvt_pk_f16_f32 v146, v140, v141\n\tv_cvt_pk_f16_f32 v147, v142, v143\n\tv_pk_fma_f32 v[120:121], v[136:137], %3, v[120:121] op_sel:[1,1,0] op_sel_hi:[0,1,1] neg_lo:[1,0,0]\n\tv_pk_fma_f32 v[122:123], v[138:139], %3, v[122:123] op_sel:[1,1,0] op_sel_hi:[0,1,1] neg_lo:[1,0,0]\n\tv_pk_fma_f32 v[126:127], v[140:141], %3, v[126:127] op_sel:[1,1,0] op_sel_hi:[0,1,1] neg_lo:[1,0,0]\n\tv_pk_fma_f32 v[128:129], v[124:125], %5, v[128:129] op_sel:[1,1,0] op_sel_hi:[0,1,1] neg_lo:[1,0,0]\n\tds_write_addtid_b32 v144 offset:1088\n\tds_write_addtid_b32 v145 offset:1360\n\tds_write_addtid_b32 v146 offset:2176\n\tds_write_addtid_b32 v147 offset:4896\n\tv_pk_mul_f32 v[130:131], v[126:127], %3 op_sel_hi:[1,0]\n\tv_pk_mul_f32 v[136:137], v[128:129], %4 op_sel_hi:[1,0]\n\tv_pk_mul_f32 v[138:139], v[132:133], %6 op_sel_hi:[1,0]\n\tv_pk_mul_f32 v[140:141], v[134:135], %6 op_sel_hi:[1,0]\n\tv_cvt_pk_f16_f32 v148, v120, v121\n\tv_cvt_pk_f16_f32 v149, v122, v123\n\tv_cvt_pk_f16_f32 v150, v126, v127\n\tv_cvt_pk_f16_f32 v151, v128, v129\n\tv_pk_fma_f32 v[130:131], v[126:127], %3, v[130:131] op_sel:[1,1,0] op_sel_hi:[0,1,1] neg_lo:[1,0,0]\n\tv_pk_fma_f32 v[136:137], v[128:129], %4, v[136:137] op_sel:[1,1,0] op_sel_hi:[0,1,1] neg_lo:[1,0,0]\n\tv_pk_fma_f32 v[138:139], v[132:133], %6, v[138:139] op_sel:[1,1,0] op_sel_hi:[0,1,1] neg_lo:[1,0,0]\n\tv_pk_fma_f32 v[140:141], v[134:135], %6, v[140:141] op_sel:[1,1,0] op_sel_hi:[0,1,1] neg_lo:[1,0,0]\n\tds_write_addtid_b32 v148 offset:1632\n\tds_write_addtid_b32 v149 offset:1904\n\tds_write_addtid_b32 v150 offset:3264\n\tds_write_addtid_b32 v151 offset:3808\n\tv_cvt_pk_f16_f32 v144, v130, v131\n\tv_cvt_pk_f16_f32 v145, v136, v137\n\tv_cvt_pk_f16_f32 v146, v138, v139\n\tv_cvt_pk_f16_f32 v147, v140, v141\n\tds_write_addtid_b32 v144 offset:2448\n\tds_write_addtid_b32 v145 offset:5984\n\tds_write_addtid_b32 v146 offset:5440\n\tds_write_addtid_b32 v147 offset:4624"
#define XTAB_CLOB "v120", "v121", "v122", "v123", "v124", "v125", "v126", "v127", "v128", "v129", "v130", "v131", "v132", "v133", "v134", "v135", "v136", "v137", "v138", "v139", "v140", "v141", "v142", "v143", "v144", "v145", "v146", "v147", "v148", "v149", "v150", "v151"

#define CONV_EVEN "s_waitcnt lgkmcnt(8)\n\tds_read_b32 %18, %29\n\tv_mfma_f32_16x16x32_bf16 %17, %0, %8, %17\n\tds_read_b128 %8, %28 offset:1024\n\tv_mfma_f32_16x16x32_bf16 %16, %0, %19, %16\n\tds_read_b128 %0, %27 offset:512\n\tv_mfma_f32_16x16x32_bf16 %17, %1, %9, %17\n\tds_read_b128 %9, %28 offset:1088\n\tv_mfma_f32_16x16x32_bf16 %16, %1, %20, %16\n\tds_read_b128 %1, %27 offset:576\n\tv_mfma_f32_16x16x32_bf16 %17, %2, %10, %17\n\tds_read_b128 %10, %28 offset:1152\n\tv_mfma_f32_16x16x32_bf16 %16, %2, %21, %16\n\tds_read_b128 %2, %27 offset:640\n\tv_mfma_f32_16x16x32_bf16 %17, %3, %11, %17\n\tds_read_b128 %11, %28 offset:1216\n\tv_mfma_f32_16x16x32_bf16 %16, %3, %22, %16\n\tds_read_b128 %3, %27 offset:704\n\ts_waitcnt lgkmcnt(8)\n\tv_mfma_f32_16x16x32_bf16 %17, %4, %12, %17\n\tds_read_b128 %12, %28 offset:1280\n\tv_mfma_f32_16x16x32_bf16 %16, %4, %23, %16\n\tds_read_b128 %4, %27 offset:768\n\tv_mfma_f32_16x16x32_bf16 %17, %5, %13, %17\n\tds_read_b128 %13, %28 offset:1344\n\tv_mfma_f32_16x16x32_bf16 %16, %5, %24, %16\n\tds_read_b128 %5, %27 offset:832\n\tv_mfma_f32_16x16x32_bf16 %17, %6, %14, %17\n\tds_read_b128 %14, %28 offset:1408\n\tv_mfma_f32_16x16x32_bf16 %16, %6, %25, %16\n\tds_read_b128 %6, %27 offset:896\n\tv_mfma_f32_16x16x32_bf16 %17, %7, %15, %17\n\tds_read_b128 %15, %28 offset:1472\n\tv_mfma_f32_16x16x32_bf16 %16, %7, %26, %16\n\tds_read_b128 %7, %27 offset:960"
#define CONV_ODD "s_waitcnt lgkmcnt(8)\n\tds_read_b32 %18, %29\n\tv_mfma_f32_16x16x32_bf16 %17, %0, %8, %17\n\tds_read_b128 %8, %28 offset:1536\n\tv_mfma_f32_16x16x32_bf16 %16, %0, %19, %16\n\tds_read_b128 %0, %27 offset:1024\n\tv_mfma_f32_16x16x32_bf16 %17, %1, %9, %17\n\tds_read_b128 %9, %28 offset:1600\n\tv_mfma_f32_16x16x32_bf16 %16, %1, %20, %16\n\tds_read_b128 %1, %27 offset:1088\n\tv_mfma_f32_16x16x32_bf16 %17, %2, %10, %17\n\tds_read_b128 %10, %28 offset:1664\n\tv_mfma_f32_16x16x32_bf16 %16, %2, %21, %16\n\tds_read_b128 %2, %27 offset:1152\n\tv_mfma_f32_16x16x32_bf16 %17, %3, %11, %17\n\tds_read_b128 %11, %28 offset:1728\n\tv_mfma_f32_16x16x32_bf16 %16, %3, %22, %16\n\tds_read_b128 %3, %27 offset:1216\n\ts_waitcnt lgkmcnt(8)\n\tv_mfma_f32_16x16x32_bf16 %17, %4, %12, %17\n\tds_read_b128 %12, %28 offset:1792\n\tv_mfma_f32_16x16x32_bf16 %16, %4, %23, %16\n\tds_read_b128 %4, %27 offset:1280\n\tv_mfma_f32_16x16x32_bf16 %17, %5, %13, %17\n\tds_read_b128 %13, %28 offset:1856\n\tv_mfma_f32_16x16x32_bf16 %16, %5, %24, %16\n\tds_read_b128 %5, %27 offset:1344\n\tv_mfma_f32_16x16x32_bf16 %17, %6, %14, %17\n\tds_read_b128 %14, %28 offset:1920\n\tv_mfma_f32_16x16x32_bf16 %16, %6, %25, %16\n\tds_read_b128 %6, %27 offset:1408\n\tv_mfma_f32_16x16x32_bf16 %17, %7, %15, %17\n\tds_read_b128 %15, %28 offset:1984\n\tv_mfma_f32_16x16x32_bf16 %16, %7, %26, %16\n\tds_read_b128 %7, %27 offset:1472"
#define CONV_LAST_EVEN "s_waitcnt lgkmcnt(8)\n\tds_read_b32 %18, %29\n\tv_mfma_f32_16x16x32_bf16 %17, %0, %8, %17\n\tv_mfma_f32_16x16x32_bf16 %16, %0, %19, %16\n\tds_read_b128 %0, %27 offset:512\n\tv_mfma_f32_16x16x32_bf16 %17, %1, %9, %17\n\tv_mfma_f32_16x16x32_bf16 %16, %1, %20, %16\n\tds_read_b128 %1, %27 offset:576\n\tv_mfma_f32_16x16x32_bf16 %17, %2, %10, %17\n\tv_mfma_f32_16x16x32_bf16 %16, %2, %21, %16\n\tds_read_b128 %2, %27 offset:640\n\tv_mfma_f32_16x16x32_bf16 %17, %3, %11, %17\n\tv_mfma_f32_16x16x32_bf16 %16, %3, %22, %16\n\tds_read_b128 %3, %27 offset:704\n\ts_waitcnt lgkmcnt(4)\n\tv_mfma_f32_16x16x32_bf16 %17, %4, %12, %17\n\tv_mfma_f32_16x16x32_bf16 %16, %4, %23, %16\n\tds_read_b128 %4, %27 offset:768\n\tv_mfma_f32_16x16x32_bf16 %17, %5, %13, %17\n\tv_mfma_f32_16x16x32_bf16 %16, %5, %24, %16\n\tds_read_b128 %5, %27 offset:832\n\tv_mfma_f32_16x16x32_bf16 %17, %6, %14, %17\n\tv_mfma_f32_16x16x32_bf16 %16, %6, %25, %16\n\tds_read_b128 %6, %27 offset:896\n\tv_mfma_f32_16x16x32_bf16 %17, %7, %15, %17\n\tv_mfma_f32_16x16x32_bf16 %16, %7, %26, %16\n\tds_read_b128 %7, %27 offset:960"
#define CONV_FINAL "s_waitcnt lgkmcnt(4)\n\tv_mfma_f32_16x16x32_bf16 %16, %0, %8, %16\n\tv_mfma_f32_16x16x32_bf16 %16, %1, %9, %16\n\tv_mfma_f32_16x16x32_bf16 %16, %2, %10, %16\n\tv_mfma_f32_16x16x32_bf16 %16, %3, %11, %16\n\ts_waitcnt lgkmcnt(0)\n\tv_mfma_f32_16x16x32_bf16 %16, %4, %12, %16\n\tv_mfma_f32_16x16x32_bf16 %16, %5, %13, %16\n\tv_mfma_f32_16x16x32_bf16 %16, %6, %14, %16\n\tv_mfma_f32_16x16x32_bf16 %16, %7, %15, %16\n\ts_nop 7"
#define CONV_BLOCK(STR, X, Y) asm volatile(STR : "+v"(A[0]), "+v"(A[1]), "+v"(A[2]), "+v"(A[3]), "+v"(A[4]), "+v"(A[5]), "+v"(A[6]), "+v"(A[7]), \
    "+v"(X[0]), "+v"(X[1]), "+v"(X[2]), "+v"(X[3]), "+v"(X[4]), "+v"(X[5]), "+v"(X[6]), "+v"(X[7]), "+v"(c0), "+v"(c1), "=&v"(fl) \
    : "v"(Y[0]), "v"(Y[1]), "v"(Y[2]), "v"(Y[3]), "v"(Y[4]), "v"(Y[5]), "v"(Y[6]), "v"(Y[7]), "v"(pa), "v"(pb), "v"(afl) : "memory")
#define NEED_CHUNK(c) do { const unsigned mk_ = 1u << (8 * ((c) - 4)); \
    if (!((unsigned)__builtin_amdgcn_readfirstlane(fl) & mk_)) { \
      asm volatile("" ::: "memory"); \
      while (!((unsigned)__builtin_amdgcn_readfirstlane(*(volatile LAS unsigned*)(flags + 8)) & mk_)) __builtin_amdgcn_s_sleep(1); \
      asm volatile("" ::: "memory"); } } while (0)

__device__ __forceinline__ void psum4(unsigned a0, unsigned a1, unsigned a2, unsigned a3, float& s0, float& s1, float& s2, float& s3) {
  f2 p0, q0, p1, q1, p2, q2, p3, q3;
  asm volatile(
      "ds_read2st64_b32 %0, %8 offset1:68\n\tds_read2st64_b32 %1, %8 offset0:136 offset1:204\n\t"
      "ds_read2st64_b32 %2, %9 offset1:68\n\tds_read2st64_b32 %3, %9 offset0:136 offset1:204\n\t"
      "ds_read2st64_b32 %4, %10 offset1:68\n\tds_read2st64_b32 %5, %10 offset0:136 offset1:204\n\t"
      "ds_read2st64_b32 %6, %11 offset1:68\n\tds_read2st64_b32 %7, %11 offset0:136 offset1:204\n\t"
      "s_waitcnt lgkmcnt(6)\n\tv_pk_add_f32 %0, %0, %1\n\t"
      "s_waitcnt lgkmcnt(4)\n\tv_pk_add_f32 %2, %2, %3\n\t"
      "s_waitcnt lgkmcnt(2)\n\tv_pk_add_f32 %4, %4, %5\n\t"
      "s_waitcnt lgkmcnt(0)\n\tv_pk_add_f32 %6, %6, %7"
      : "=&v"(p0), "=&v"(q0), "=&v"(p1), "=&v"(q1), "=&v"(p2), "=&v"(q2), "=&v"(p3), "=&v"(q3)
      : "v"(a0), "v"(a1), "v"(a2), "v"(a3) : "memory");
  s0 = p0[0] + p0[1]; s1 = p1[0] + p1[1]; s2 = p2[0] + p2[1]; s3 = p3[0] + p3[1];
}
__device__ __forceinline__ void st11(unsigned base, const float* v) {
  asm volatile("s_mov_b32 m0, %0\n\ts_nop 0\n\t"
               "ds_write_addtid_b32 %1 offset:0\n\tds_write_addtid_b32 %2 offset:256\n\tds_write_addtid_b32 %3 offset:512\n\t"
               "ds_write_addtid_b32 %4 offset:768\n\tds_write_addtid_b32 %5 offset:1024\n\tds_write_addtid_b32 %6 offset:1280\n\t"
               "ds_write_addtid_b32 %7 offset:1536\n\tds_write_addtid_b32 %8 offset:1792\n\tds_write_addtid_b32 %9 offset:2048\n\t"
               "ds_write_addtid_b32 %10 offset:2304\n\tds_write_addtid_b32 %11 offset:2560"
               :: "s"(base), "v"(v[0]), "v"(v[1]), "v"(v[2]), "v"(v[3]), "v"(v[4]), "v"(v[5]), "v"(v[6]), "v"(v[7]), "v"(v[8]), "v"(v[9]), "v"(v[10]) : "memory");
}

__device__ __forceinline__ void build_chunk(LAS char* lds, int c, int lane, const unsigned* d, const unsigned* pe) {
  const unsigned d0 = d[0], d1 = d[1], d2 = d[2], d3 = d[3];
  *(LAS u4*)(lds + RAW_OFF + (256 * c + 4 * lane) * 4) = u4{d0, d1, d2, d3};
  const int m0 = 256 * c + 4 * lane + 16;
#pragma unroll
  for (int ch = 0; ch < 2; ch++) {
    const unsigned sel = ch ? 0x07060302u : 0x05040100u;
    const unsigned p0 = __builtin_amdgcn_perm(d1, d0, sel), p1 = __builtin_amdgcn_perm(d3, d2, sel);
    const unsigned q0 = __builtin_amdgcn_perm(pe[1], pe[0], sel), q1 = __builtin_amdgcn_perm(pe[3], pe[2], sel);
    const unsigned P0 = __builtin_amdgcn_update_dpp(q0, p0, 0x138, 0xf, 0xf, false);
    const unsigned P1 = __builtin_amdgcn_update_dpp(q1, p1, 0x138, 0xf, 0xf, false);
    const unsigned a0 = __builtin_amdgcn_alignbit(P1, P0, 16), a1 = __builtin_amdgcn_alignbit(p0, P1, 16), a2 = __builtin_amdgcn_alignbit(p1, p0, 16);
    LAS char* base = lds + U_OFF + ch * U_CH + 2 * m0;
    *(LAS u2*)(base + copy_base(0)) = u2{p0, p1};
    *(LAS u2*)(base + copy_base(1) - 8) = u2{a0, a1};
    *(LAS u2*)(base + copy_base(2) - 8) = u2{P1, p0};
    *(LAS u2*)(base + copy_base(3) - 8) = u2{a1, a2};
    *(LAS u2*)(base + copy_base(4) - 8) = u2{p0, p1};
    *(LAS u2*)(base + copy_base(5) - 16) = u2{a0, a1};
    *(LAS u2*)(base + copy_base(6) - 16) = u2{P1, p0};
    *(LAS u2*)(base + copy_base(7) - 16) = u2{a1, a2};
  }
}

__global__ __launch_bounds__(768, 3) void s4d_kernel(const unsigned* __restrict__ u32p, const unsigned short* __restrict__ logA,
                                                     const unsigned short* __restrict__ Aim, const unsigned short* __restrict__ Cr,
                                                     const unsigned short* __restrict__ Ci, const unsigned short* __restrict__ logdt,
                                                     const unsigned short* __restrict__ Dsk, unsigned* __restrict__ out
) {
  __shared__ __attribute__((aligned(16))) char smem[LDS_BYTES];
  LAS char* lds = (LAS char*)smem;
  LAS unsigned* flags = (LAS unsigned*)(lds + FLAG_OFF);
  const int b = blockIdx.x;
  if ((b & 7) == 7) return;
  const int cp = (b & 7) * 32 + (b >> 3);
  const int lane = threadIdx.x & 63;
  const int w = __builtin_amdgcn_readfirstlane(threadIdx.x >> 6);

  if (w >= 8) {
    const int j = w - 8;
    unsigned dv[2][4], pe[2][4];
    asm volatile("s_barrier" ::: "memory");
    {
      const unsigned* p = u32p + (size_t)(256 * j + 4 * lane) * (D_ / 2) + cp;
#pragma unroll
      for (int i = 0; i < 4; i++) dv[0][i] = p[i * (D_ / 2)];
      asm volatile("s_sleep 3" ::: "memory");
#pragma unroll
      for (int i = 0; i < 4; i++) dv[1][i] = p[(1024 + i) * (D_ / 2)];
    }
#pragma unroll
    for (int r = 0; r < 2; r++) {
      const int c = j + 4 * r;
#pragma unroll
      for (int i = 0; i < 4; i++) pe[r][i] = (c > 0) ? u32p[(size_t)(256 * c - 4 + i) * (D_ / 2) + cp] : 0u;
    }
    if (j == 0) {
      if (lane < 16) flags[lane] = 0;
      *(LAS u2*)(lds + U_OFF + (lane >> 5) * U_CH + copy_base((lane >> 2) & 7) + (lane & 3) * 8) = u2{0u, 0u};
    }
    asm volatile("" : "+v"(dv[0][0]), "+v"(dv[0][1]), "+v"(dv[0][2]), "+v"(dv[0][3]));
    __builtin_amdgcn_s_setprio(2);
    build_chunk(lds, j, lane, dv[0], pe[0]);
    __builtin_amdgcn_s_setprio(0);
    wg_barrier();
    wg_barrier();
    asm volatile("" : "+v"(dv[1][0]), "+v"(dv[1][1]), "+v"(dv[1][2]), "+v"(dv[1][3]));
    __builtin_amdgcn_s_setprio(1);
    build_chunk(lds, j + 4, lane, dv[1], pe[1]);
    asm volatile("s_waitcnt lgkmcnt(0)" ::: "memory");
    *((volatile LAS unsigned char*)(flags + 8) + j) = 1;
    return;
  }

  const int ch = w & 1, mg = w >> 1;
  const int d = 2 * cp + ch;
  const int pidx = d * N_ + 64 * mg + lane;
  unsigned la = logA[pidx], ai = Aim[pidx], cr = Cr[pidx], ci = Ci[pidx];
  unsigned ldt = logdt[d];
  const unsigned ds0 = Dsk[2 * cp], ds1 = Dsk[2 * cp + 1];

  const u4 at = *(const u4*)&ADDR.v[mg][lane][0];
  asm volatile("s_barrier" ::: "memory");
  const unsigned tabb = (unsigned)(uintptr_t)(lds + w * TAB_REGION);
  const int col = lane & 31, h = lane >> 5;
  const int tau = (mg < 2) ? 3 - mg : mg - 2;
  const int t0 = 2 * tau;
  if (w >= 4) __builtin_amdgcn_s_setprio(1);

  const unsigned lbase = (unsigned)(uintptr_t)lds;
  unsigned ad0 = lbase + ch * TAB_REGION + (at[0] & 0xffffu), ad1 = lbase + ch * TAB_REGION + (at[0] >> 16);
  unsigned ad2 = lbase + ch * TAB_REGION + (at[1] & 0xffffu), ad3 = lbase + ch * TAB_REGION + (at[1] >> 16);
  unsigned adR = lbase + ch * TAB_REGION + (at[2] & 0xffffu);
  const LAS char* pa = lds + U_OFF + ch * U_CH + (at[3] & 0xffffu);
  const LAS char* pb = lds + ch * TAB_REGION + (at[3] >> 16);
  asm volatile("" : "+v"(ad0), "+v"(ad1), "+v"(ad2), "+v"(ad3), "+v"(adR), "+v"(pa), "+v"(pb));
  float unscale;
  f16v acc;
  asm volatile("" : "+v"(la), "+v"(ai), "+v"(cr), "+v"(ci), "+v"(ldt));
  {
    const float dt = rbf(exp_acc(bf2f(ldt)));
    const float e = rbf(exp_acc(bf2f(la)));
    const float a2 = -(e * dt) * 1.44269504088896f;
    const float th = (bf2f(ai) * dt) * 0.159154943091895f;
    f2 C = {bf2f(cr) * dt, bf2f(ci) * dt};
    unsigned mx = max(__float_as_uint(C[0]) & 0x7fffffffu, __float_as_uint(C[1]) & 0x7fffffffu);
    mx = max(mx, (unsigned)__builtin_amdgcn_update_dpp(0, (int)mx, 0x111, 0xf, 0xf, true));
    mx = max(mx, (unsigned)__builtin_amdgcn_update_dpp(0, (int)mx, 0x112, 0xf, 0xf, true));
    mx = max(mx, (unsigned)__builtin_amdgcn_update_dpp(0, (int)mx, 0x114, 0xf, 0xf, true));
    mx = max(mx, (unsigned)__builtin_amdgcn_update_dpp(0, (int)mx, 0x118, 0xf, 0xf, true));
    mx = max(mx, (unsigned)__builtin_amdgcn_update_dpp(0, (int)mx, 0x142, 0xa, 0xf, true));
    mx = max(mx, (unsigned)__builtin_amdgcn_update_dpp(0, (int)mx, 0x143, 0xc, 0xf, true));
    unsigned E = (unsigned)__builtin_amdgcn_readlane((int)mx, 63) >> 23;
    E = min(max(E, 1u), 253u);
    const float sc = __uint_as_float((254u - E) << 23);
    unscale = __uint_as_float((E + 1u) << 23);
    C[0] *= sc; C[1] *= sc;

    const f2 w1 = cexp_p(a2, th, 1.0f);
    const f2 w256 = cexp_p(a2, th, 256.0f);
    const f2 w2 = cmul(w1, w1), w4 = cmul(w2, w2);
    f2 v = cmul(w4, w4);
    v[1] = -v[1];
    const f2 v2 = cmul(v, v), v3 = cmul(v2, v), v4 = cmul(v2, v2);
    const f2 w512 = cmul(w256, w256), w1024 = cmul(w512, w512);
    const LAS char* ta = lds + w * TAB_REGION + col * TAB_STRIDE + h * 16;
    h8 Bf[8];
    asm volatile(YTAB_ASM :: "s"(tabb), "v"(v4), "v"(pk_f16(v)), "v"(pk_f16(v2)), "v"(pk_f16(v3)), "v"(0x00003c00u)
                 : "memory", YTAB_CLOB);
#pragma unroll
    for (int ks = 0; ks < 8; ks++) Bf[ks] = *(const LAS h8*)(ta + ks * 32);
    if (w >= 4) __builtin_amdgcn_s_setprio(0); else __builtin_amdgcn_s_setprio(1);
    asm volatile(XTAB_ASM :: "s"(tabb), "v"(C), "v"(w1), "v"(w2), "v"(w4), "v"(w256), "v"(w512), "v"(w1024), "v"(pk_f16(C))
                 : "memory", XTAB_CLOB);
#pragma unroll
    for (int i = 0; i < 16; i++) acc[i] = 0.f;
#pragma unroll
    for (int ks = 0; ks < 8; ks++) {
      const h8 A = *(const LAS h8*)(ta + ks * 32);
      acc = __builtin_amdgcn_mfma_f32_32x32x16_f16(A, Bf[ks], acc, 0, 0, 0);
    }
  }
  b8 A[8];
  {
    float pv[11];
#pragma unroll
    for (int i = 0; i < 11; i++) pv[i] = acc[i] * unscale;
    st11(tabb, pv);
    wg_barrier();
    float k0, k1, k2, k3;
    psum4(ad0, ad1, ad2, ad3, k0, k1, k2, k3);
    LAS char* rp = (LAS char*)(uintptr_t)adR;
    if (mg == 0) {
      *(LAS u2*)rp = u2{pk_bf16(k3, k2), pk_bf16(k1, k0)};
      if (lane < 32) *(LAS u4*)(lds + (2 + ch) * TAB_REGION + R_OFF + 4096 + lane * 16) = u4{0u, 0u, 0u, 0u};
    } else if (mg == 1) {
      *(LAS u2*)rp = u2{pk_bf16(k2, k1), pk_bf16(k0, k0)};
    } else if (mg == 2) {
      *(LAS u4*)rp = u4{pk_bf16(k2, k2), pk_bf16(k1, k1), pk_bf16(k1, k0), pk_bf16(k0, k0)};
    } else {
      const unsigned p00 = pk_bf16(k0, k0), p11 = pk_bf16(k1, k1), p22 = pk_bf16(k2, k2), p10 = pk_bf16(k1, k0);
      *(LAS u4*)rp = u4{p22, p22, p11, p11};
      *(LAS u4*)(rp + 16) = u4{p11, p10, p00, p00};
    }
    wg_barrier();
  }
  if (tau == 0) __builtin_amdgcn_s_setprio(3); else if (tau == 1) __builtin_amdgcn_s_setprio(2); else if (tau == 2) __builtin_amdgcn_s_setprio(1); else __builtin_amdgcn_s_setprio(0);

  {
    const int a = lane & 15, g = lane >> 4;
    f4 c0 = {0.f, 0.f, 0.f, 0.f}, c1 = {0.f, 0.f, 0.f, 0.f};
    b8 Bx[8], By[8];
#pragma unroll
    for (int s = 0; s < 8; s++) A[s] = *(const LAS b8*)(pa + 64 * s);
#pragma unroll
    for (int s = 0; s < 8; s++) { Bx[s] = *(const LAS b8*)(pb + 64 * s); By[s] = *(const LAS b8*)(pb + 512 + 64 * s); }
    asm volatile("s_nop 1" : "+v"(A[0]), "+v"(A[1]), "+v"(A[2]), "+v"(A[3]), "+v"(A[4]), "+v"(A[5]), "+v"(A[6]), "+v"(A[7]),
                 "+v"(Bx[0]), "+v"(Bx[1]), "+v"(Bx[2]), "+v"(Bx[3]), "+v"(Bx[4]), "+v"(Bx[5]), "+v"(Bx[6]), "+v"(Bx[7]), "+v"(c0), "+v"(c1),
                 "+v"(By[0]), "+v"(By[1]), "+v"(By[2]), "+v"(By[3]), "+v"(By[4]), "+v"(By[5]), "+v"(By[6]), "+v"(By[7]));
    const unsigned afl = (unsigned)(uintptr_t)(flags + 8);
    unsigned fl = 0;
    for (int m = 0; m < t0; m += 2) {
      if (m >= 4) NEED_CHUNK(m + 1);
      CONV_BLOCK(CONV_EVEN, Bx, By);
      if (m >= 2) NEED_CHUNK(m + 2);
      CONV_BLOCK(CONV_ODD, By, Bx);
      pa += 1024; pb += 1024;
    }
    if (t0 >= 4) NEED_CHUNK(t0 + 1);
    const int l0 = 256 * (t0 + ch) + 16 * a + 4 * g;
    const u4 uu = *(const LAS u4*)(lds + RAW_OFF + l0 * 4);
    CONV_BLOCK(CONV_LAST_EVEN, Bx, By);
    if (ch) {
      asm volatile("s_nop 7" : "+v"(c0));
      *(LAS f4*)(lds + w * TAB_REGION + lane * 16) = c0;
      asm volatile("" ::: "memory");
      *(volatile LAS unsigned*)(flags + w) = 1u;
      asm volatile("" ::: "memory");
    }
    asm volatile(CONV_FINAL : "+v"(A[0]), "+v"(A[1]), "+v"(A[2]), "+v"(A[3]), "+v"(A[4]), "+v"(A[5]), "+v"(A[6]), "+v"(A[7]),
                 "+v"(By[0]), "+v"(By[1]), "+v"(By[2]), "+v"(By[3]), "+v"(By[4]), "+v"(By[5]), "+v"(By[6]), "+v"(By[7]), "+v"(c1), "+v"(c0) :: "memory");
    if (!ch) {
      *(LAS f4*)(lds + w * TAB_REGION + lane * 16) = c1;
      asm volatile("" ::: "memory");
      *(volatile LAS unsigned*)(flags + w) = 1u;
      asm volatile("" ::: "memory");
    }
    const f4 mine = ch ? c1 : c0;
    const float D0 = bf2f(ds0), D1 = bf2f(ds1);
    unsigned sk[4];
#pragma unroll
    for (int r = 0; r < 4; r++) sk[r] = pk_bf16(__uint_as_float(uu[r] << 16) * D0, __uint_as_float(uu[r] & 0xffff0000u) * D1);
    f4 other;
    {
      const unsigned af = (unsigned)(uintptr_t)(flags + (w ^ 1)), ao = (unsigned)(uintptr_t)(lds + (w ^ 1) * TAB_REGION + lane * 16);
      unsigned fl;
      do {
        asm volatile("ds_read_b32 %0, %2\n\tds_read_b128 %1, %3\n\ts_waitcnt lgkmcnt(0)" : "=&v"(fl), "=&v"(other) : "v"(af), "v"(ao) : "memory");
      } while (__builtin_amdgcn_readfirstlane(fl) == 0);
    }
    unsigned* op = out + (size_t)l0 * (D_ / 2) + cp;
#pragma unroll
    for (int r = 0; r < 4; r++) {
      const unsigned y = ch ? pk_bf16(other[r], mine[r]) : pk_bf16(mine[r], other[r]);
      const float o0 = __uint_as_float(y << 16) + __uint_as_float(sk[r] << 16);
      const float o1 = __uint_as_float(y & 0xffff0000u) + __uint_as_float(sk[r] & 0xffff0000u);
      op[r * (D_ / 2)] = pk_bf16(o0, o1);
    }
  }
}

extern "C" void kernel_launch(void* const* d_in, const int* in_sizes, int n_in, void* d_out, int out_size, void* d_ws,
                              size_t ws_size, hipStream_t stream) {
  s4d_kernel<<<256, 768, 0, stream>>>((const unsigned*)d_in[0], (const unsigned short*)d_in[1], (const unsigned short*)d_in[2],
                                      (const unsigned short*)d_in[3], (const unsigned short*)d_in[4], (const unsigned short*)d_in[5],
                                      (const unsigned short*)d_in[6], (unsigned*)d_out
  );
}
```
